```python
import torch

def _softmax(x, axis=-1):
    return torch.softmax(x, dim=axis)
def _gelu(x):
    return torch.nn.functional.gelu(x, approximate="tanh")
def _silu(x):
    return torch.nn.functional.silu(x)
def _sigmoid(x):
    return torch.sigmoid(x)
def _layernorm(x, axis=-1, eps=1e-5):
    m = x.mean(dim=axis, keepdim=True)
    v = x.var(dim=axis, keepdim=True, unbiased=False)
    return (x - m) / torch.sqrt(v + eps)
def _scatter_add(tgt, idx, src, axis=0):
    return tgt.clone().index_add_(axis, idx, src)
def _segment_sum(src, seg, n):
    out = torch.zeros((n,) + src.shape[1:], dtype=src.dtype)
    return out.index_add_(0, seg, src)
def _pad(x, pw):
    flat = tuple(v for pair in reversed(pw) for v in pair)
    return torch.nn.functional.pad(x, flat)

kernel_name = 'gather_scatter__edge_featured_mpnn_mpnn__graph'

def setup_inputs():

    _w = (lambda a: (lambda t: t.to(torch.bfloat16) if t.is_floating_point() else t)(torch.as_tensor(a)))
    N, deg, D, De = 10000, 32, 128, max(0, 1)
    E = N * deg
    H = 2 * D
    return {
        "x": _w((lambda *s: torch.randn(*s, dtype=torch.bfloat16) * 0.05)(N, D)),
        "e_attr": _w((lambda *s: torch.randn(*s, dtype=torch.bfloat16) * 1.6)(E, De)),
        "src": _w((lambda lo, hi, *s: torch.randint(lo, hi, s))(0, N, E).to(torch.int64)),
        "dst": _w((lambda lo, hi, *s: torch.randint(lo, hi, s))(0, N, E).to(torch.int64)),
        "w1": _w((lambda *s: torch.randn(*s, dtype=torch.bfloat16) * 1.6)(2 * D + De, H) * (1.0 / (2 * D + De) ** 0.5)),
        "b1": _w((lambda *s: torch.randn(*s, dtype=torch.bfloat16) * 0.8)(H) * 0.01),
        "w2": _w((lambda *s: torch.randn(*s, dtype=torch.bfloat16) * 0.05)(H, D) * (1.0 / H ** 0.5)),
        "b2": _w((lambda *s: torch.randn(*s, dtype=torch.bfloat16) * 0.05)(D) * 0.01),
    }

def reference(x, e_attr, src, dst, w1, b1, w2, b2):
    N, D = x.shape
    h_src = x[src]
    h_dst = x[dst]
    m_in = torch.concatenate([h_src, h_dst, e_attr], axis=-1)
    h = _silu(m_in @ w1 + b1)
    msg = h @ w2 + b2
    return _segment_sum(msg, dst, N)
```

```cpp
#include <hip/hip_runtime.h>
#include <hip/hip_bf16.h>
#include <stdint.h>

using bf16x8 = __attribute__((ext_vector_type(8))) short;
using f32x4  = __attribute__((ext_vector_type(4))) float;

namespace {

constexpr int NN  = 10000;
constexpr int EE  = 320000;
constexpr int DD  = 128;
constexpr int HH  = 256;
constexpr int G   = 64;
constexpr int CH  = EE / G;
constexpr int OFFS_STRIDE = 10008;
constexpr int GEMM_TG = 40;
constexpr int K1_BLOCKS = G + 2 * GEMM_TG + 1;
constexpr int WT_STRIDE = 136;

constexpr size_t WS_P    = 0;
constexpr size_t WS_Q    = WS_P + (size_t)NN * HH * 2;
constexpr size_t WS_OFFS = WS_Q + (size_t)NN * HH * 4;
constexpr size_t WS_LIST = WS_OFFS + (size_t)G * OFFS_STRIDE * 2;
constexpr size_t WS_W2T  = WS_LIST + (size_t)G * CH * 4;
constexpr size_t WS_END  = WS_W2T + (size_t)DD * HH * 2;

constexpr float NEG_LOG2E = -1.4426950408889634f;
constexpr float NEG_LN2   = -0.6931471805599453f;

__device__ __forceinline__ float bf2f(unsigned short u) { return __uint_as_float(((unsigned)u) << 16); }
__device__ __forceinline__ unsigned short f2bf(float f) {
  __hip_bfloat16 h = __float2bfloat16(f);
  return *reinterpret_cast<unsigned short*>(&h);
}
__device__ __forceinline__ unsigned pack2(float a, float b) {
  return (unsigned)f2bf(a) | ((unsigned)f2bf(b) << 16);
}

constexpr int K1_LDS = HH * WT_STRIDE * 2;

__global__ __launch_bounds__(1024) void k1_prep(
    const unsigned short* __restrict__ x, const unsigned short* __restrict__ eattr,
    const int* __restrict__ src, const int* __restrict__ dst,
    const unsigned short* __restrict__ w1, const unsigned short* __restrict__ b1,
    const unsigned short* __restrict__ w2,
    unsigned short* __restrict__ Pp, float* __restrict__ Qp,
    unsigned short* __restrict__ offs, unsigned* __restrict__ list,
    unsigned short* __restrict__ w2T) {
  __shared__ __attribute__((aligned(16))) unsigned char smem[K1_LDS];
  const int tid = threadIdx.x;
  const int lane = tid & 63;
  const int wave = tid >> 6;
  const int bid = blockIdx.x;

  if (bid < G) {
    unsigned* hist = reinterpret_cast<unsigned*>(smem);
    unsigned short* lst = reinterpret_cast<unsigned short*>(smem + 40064);
    unsigned* wsum = reinterpret_cast<unsigned*>(smem + 50064);
    for (int i = tid; i < NN + 1; i += 1024) hist[i] = 0u;
    __syncthreads();
    int d[5];
    unsigned r[5];
    const int ebase = bid * CH;
#pragma unroll
    for (int i = 0; i < 5; ++i) {
      const int idx = i * 1024 + tid;
      d[i] = 0; r[i] = 0;
      if (idx < CH) {
        d[i] = dst[ebase + idx];
        r[i] = atomicAdd(&hist[d[i]], 1u);
      }
    }
    __syncthreads();
    unsigned c[10];
    unsigned sum = 0;
    if (tid < NN / 10) {
#pragma unroll
      for (int k = 0; k < 10; ++k) { c[k] = hist[tid * 10 + k]; sum += c[k]; }
    }
    unsigned incl = sum;
#pragma unroll
    for (int o = 1; o < 64; o <<= 1) {
      unsigned t = __shfl_up(incl, o, 64);
      if (lane >= o) incl += t;
    }
    if (lane == 63) wsum[wave] = incl;
    __syncthreads();
    unsigned wpre = 0;
    for (int w = 0; w < wave; ++w) wpre += wsum[w];
    unsigned excl = wpre + incl - sum;
    if (tid < NN / 10) {
#pragma unroll
      for (int k = 0; k < 10; ++k) {
        hist[tid * 10 + k] = excl;
        offs[bid * OFFS_STRIDE + tid * 10 + k] = (unsigned short)excl;
        excl += c[k];
      }
    }
    if (tid == 0) { hist[NN] = CH; offs[bid * OFFS_STRIDE + NN] = (unsigned short)CH; }
    __syncthreads();
#pragma unroll
    for (int i = 0; i < 5; ++i) {
      const int idx = i * 1024 + tid;
      if (idx < CH) lst[hist[d[i]] + r[i]] = (unsigned short)idx;
    }
    __syncthreads();
#pragma unroll
    for (int i = 0; i < 5; ++i) {
      const int idx = i * 1024 + tid;
      if (idx < CH) {
        const unsigned s = hist[d[i]], e = hist[d[i] + 1];
        unsigned rank = 0;
        for (unsigned j = s; j < e; ++j) rank += (lst[j] < (unsigned short)idx) ? 1u : 0u;
        const unsigned rec = ((unsigned)src[ebase + idx] & 0xffffu) | ((unsigned)eattr[ebase + idx] << 16);
        list[ebase + s + rank] = rec;
      }
    }
  } else if (bid < G + 2 * GEMM_TG) {
    const int gb = bid - G;
    const int half = gb / GEMM_TG;
    const int tg = gb % GEMM_TG;
    unsigned short* WT = reinterpret_cast<unsigned short*>(smem);
#pragma unroll
    for (int i = 0; i < 4; ++i) {
      const int k = (tid >> 5) + 32 * i;
      const int c8 = (tid & 31) * 8;
      const uint4 v = *reinterpret_cast<const uint4*>(w1 + (size_t)(half * DD + k) * HH + c8);
      const unsigned vv[4] = {v.x, v.y, v.z, v.w};
#pragma unroll
      for (int j = 0; j < 4; ++j) {
        WT[(c8 + 2 * j) * WT_STRIDE + k] = (unsigned short)(vv[j] & 0xffffu);
        WT[(c8 + 2 * j + 1) * WT_STRIDE + k] = (unsigned short)(vv[j] >> 16);
      }
    }
    __syncthreads();
    const int nt = tg * 16 + wave;
    if (nt < NN / 16) {
      const int node = nt * 16 + (lane & 15);
      bf16x8 xb[4];
#pragma unroll
      for (int ks = 0; ks < 4; ++ks)
        xb[ks] = *reinterpret_cast<const bf16x8*>(x + (size_t)node * DD + ks * 32 + (lane >> 4) * 8);
      for (int ct = 0; ct < 16; ++ct) {
        f32x4 acc = {0.f, 0.f, 0.f, 0.f};
#pragma unroll
        for (int ks = 0; ks < 4; ++ks) {
          const bf16x8 a = *reinterpret_cast<const bf16x8*>(WT + (ct * 16 + (lane & 15)) * WT_STRIDE + ks * 32 + (lane >> 4) * 8);
          acc = __builtin_amdgcn_mfma_f32_16x16x32_bf16(a, xb[ks], acc, 0, 0, 0);
        }
        const int hc = ct * 16 + (lane >> 4) * 4;
        if (half == 0) {
          uint2 o;
          o.x = pack2(acc[0] * NEG_LOG2E, acc[1] * NEG_LOG2E);
          o.y = pack2(acc[2] * NEG_LOG2E, acc[3] * NEG_LOG2E);
          *reinterpret_cast<uint2*>(Pp + (size_t)node * HH + hc) = o;
        } else {
          float4 o;
          o.x = (acc[0] + bf2f(b1[hc + 0])) * NEG_LOG2E;
          o.y = (acc[1] + bf2f(b1[hc + 1])) * NEG_LOG2E;
          o.z = (acc[2] + bf2f(b1[hc + 2])) * NEG_LOG2E;
          o.w = (acc[3] + bf2f(b1[hc + 3])) * NEG_LOG2E;
          *reinterpret_cast<float4*>(Qp + (size_t)node * HH + hc) = o;
        }
      }
    }
  } else {
    for (int i = tid; i < HH * DD; i += 1024) {
      const int k = i >> 7, oc = i & 127;
      w2T[oc * HH + k] = w2[i];
    }
  }
}

constexpr int S_STRIDE = 264;
constexpr int K2_BLOCKS = 256;
constexpr int K2_MAXN = 48;

__device__ __forceinline__ float silu_t(float t) {
  const float ex = __builtin_amdgcn_exp2f(t);
  return t * __builtin_amdgcn_rcpf(1.0f + ex);
}

__global__ __launch_bounds__(1024) void k2_agg(
    const unsigned short* __restrict__ Pp, const float* __restrict__ Qp,
    const unsigned short* __restrict__ offs, const unsigned* __restrict__ list,
    const unsigned short* __restrict__ w2T, const unsigned short* __restrict__ w1,
    const unsigned short* __restrict__ b2, unsigned short* __restrict__ out) {
  __shared__ __attribute__((aligned(16))) unsigned short Shi[K2_MAXN * S_STRIDE];
  __shared__ __attribute__((aligned(16))) unsigned short Slo[K2_MAXN * S_STRIDE];
  __shared__ unsigned stag[16 * 64];
  __shared__ int degs[K2_MAXN];

  const int tid = threadIdx.x;
  const int lane = tid & 63;
  const int wave = tid >> 6;
  const int n0 = (blockIdx.x * NN) >> 8;
  const int n1 = ((blockIdx.x + 1) * NN) >> 8;
  const int nn = n1 - n0;

  float wq0, wq1, wq2, wq3;
  {
    const uint2 wv = *reinterpret_cast<const uint2*>(w1 + (size_t)(2 * DD) * HH + lane * 4);
    wq0 = __uint_as_float(wv.x << 16) * NEG_LOG2E;
    wq1 = __uint_as_float(wv.x & 0xffff0000u) * NEG_LOG2E;
    wq2 = __uint_as_float(wv.y << 16) * NEG_LOG2E;
    wq3 = __uint_as_float(wv.y & 0xffff0000u) * NEG_LOG2E;
  }
  unsigned* mystag = stag + wave * 64;

  const int wv_id = __builtin_amdgcn_readfirstlane(wave);
  for (int nl = wv_id; nl < nn; nl += 16) {
    const int n = n0 + nl;
    const int o0 = offs[lane * OFFS_STRIDE + n];
    const int o1 = offs[lane * OFFS_STRIDE + n + 1];
    const int cnt = o1 - o0;
    int incl = cnt;
#pragma unroll
    for (int o = 1; o < 64; o <<= 1) {
      int t = __shfl_up(incl, o, 64);
      if (lane >= o) incl += t;
    }
    const int start = incl - cnt;
    const int total = __builtin_amdgcn_readlane(incl, 63);
    const float4 q = *reinterpret_cast<const float4*>(Qp + (size_t)n * HH + lane * 4);
    float a0 = 0.f, a1 = 0.f, a2 = 0.f, a3 = 0.f;

    for (int w0 = 0; w0 < total; w0 += 64) {
      const int jlo = max(w0 - start, 0);
      const int jhi = min(cnt, w0 + 64 - start);
      for (int j = jlo; j < jhi; ++j) mystag[start + j - w0] = list[lane * CH + o0 + j];
      __builtin_amdgcn_wave_barrier();
      const int m = min(64, total - w0);
      unsigned rec = (lane < m) ? mystag[lane] : 0u;
      __builtin_amdgcn_wave_barrier();
      int i = 0;
      for (; i + 4 <= m; i += 4) {
        unsigned rr[4];
        uint2 v[4];
#pragma unroll
        for (int u = 0; u < 4; ++u) {
          rr[u] = __builtin_amdgcn_readlane(rec, i + u);
          v[u] = *reinterpret_cast<const uint2*>(Pp + (size_t)(rr[u] & 0xffffu) * HH + lane * 4);
        }
#pragma unroll
        for (int u = 0; u < 4; ++u) {
          const float e = __uint_as_float(rr[u] & 0xffff0000u);
          a0 += silu_t(fmaf(e, wq0, q.x) + __uint_as_float(v[u].x << 16));
          a1 += silu_t(fmaf(e, wq1, q.y) + __uint_as_float(v[u].x & 0xffff0000u));
          a2 += silu_t(fmaf(e, wq2, q.z) + __uint_as_float(v[u].y << 16));
          a3 += silu_t(fmaf(e, wq3, q.w) + __uint_as_float(v[u].y & 0xffff0000u));
        }
      }
      for (; i < m; ++i) {
        const unsigned rr = __builtin_amdgcn_readlane(rec, i);
        const uint2 v = *reinterpret_cast<const uint2*>(Pp + (size_t)(rr & 0xffffu) * HH + lane * 4);
        const float e = __uint_as_float(rr & 0xffff0000u);
        a0 += silu_t(fmaf(e, wq0, q.x) + __uint_as_float(v.x << 16));
        a1 += silu_t(fmaf(e, wq1, q.y) + __uint_as_float(v.x & 0xffff0000u));
        a2 += silu_t(fmaf(e, wq2, q.z) + __uint_as_float(v.y << 16));
        a3 += silu_t(fmaf(e, wq3, q.w) + __uint_as_float(v.y & 0xffff0000u));
      }
    }
    const float s0 = a0 * NEG_LN2, s1 = a1 * NEG_LN2, s2 = a2 * NEG_LN2, s3 = a3 * NEG_LN2;
    const unsigned short h0 = f2bf(s0), h1 = f2bf(s1), h2 = f2bf(s2), h3 = f2bf(s3);
    uint2 hv, lv;
    hv.x = (unsigned)h0 | ((unsigned)h1 << 16);
    hv.y = (unsigned)h2 | ((unsigned)h3 << 16);
    lv.x = pack2(s0 - bf2f(h0), s1 - bf2f(h1));
    lv.y = pack2(s2 - bf2f(h2), s3 - bf2f(h3));
    *reinterpret_cast<uint2*>(Shi + nl * S_STRIDE + lane * 4) = hv;
    *reinterpret_cast<uint2*>(Slo + nl * S_STRIDE + lane * 4) = lv;
    degs[nl] = total;
  }
  __syncthreads();

  const int ntiles = (nn + 15) >> 4;
  for (int t = wave; t < ntiles * 8; t += 16) {
    const int ntile = t >> 3, oct = t & 7;
    f32x4 acc = {0.f, 0.f, 0.f, 0.f};
    const int srow = ntile * 16 + (lane & 15);
#pragma unroll
    for (int ks = 0; ks < 8; ++ks) {
      const bf16x8 a = *reinterpret_cast<const bf16x8*>(w2T + (oct * 16 + (lane & 15)) * HH + ks * 32 + (lane >> 4) * 8);
      const bf16x8 bh = *reinterpret_cast<const bf16x8*>(Shi + srow * S_STRIDE + ks * 32 + (lane >> 4) * 8);
      const bf16x8 bl = *reinterpret_cast<const bf16x8*>(Slo + srow * S_STRIDE + ks * 32 + (lane >> 4) * 8);
      acc = __builtin_amdgcn_mfma_f32_16x16x32_bf16(a, bh, acc, 0, 0, 0);
      acc = __builtin_amdgcn_mfma_f32_16x16x32_bf16(a, bl, acc, 0, 0, 0);
    }
    if (srow < nn) {
      const int oc = oct * 16 + (lane >> 4) * 4;
      const float dg = (float)degs[srow];
      uint2 o;
      o.x = pack2(acc[0] + dg * bf2f(b2[oc + 0]), acc[1] + dg * bf2f(b2[oc + 1]));
      o.y = pack2(acc[2] + dg * bf2f(b2[oc + 2]), acc[3] + dg * bf2f(b2[oc + 3]));
      *reinterpret_cast<uint2*>(out + (size_t)(n0 + srow) * DD + oc) = o;
    }
  }
}

}

extern "C" void kernel_launch(void* const* d_in, const int* in_sizes, int n_in,
                              void* d_out, int out_size, void* d_ws, size_t ws_size,
                              hipStream_t stream) {
  const unsigned short* x  = (const unsigned short*)d_in[0];
  const unsigned short* ea = (const unsigned short*)d_in[1];
  const int* src = (const int*)d_in[2];
  const int* dst = (const int*)d_in[3];
  const unsigned short* w1 = (const unsigned short*)d_in[4];
  const unsigned short* b1 = (const unsigned short*)d_in[5];
  const unsigned short* w2 = (const unsigned short*)d_in[6];
  const unsigned short* b2 = (const unsigned short*)d_in[7];
  unsigned char* ws = (unsigned char*)d_ws;
  unsigned short* Pp = (unsigned short*)(ws + WS_P);
  float* Qp = (float*)(ws + WS_Q);
  unsigned short* offs = (unsigned short*)(ws + WS_OFFS);
  unsigned* list = (unsigned*)(ws + WS_LIST);
  unsigned short* w2T = (unsigned short*)(ws + WS_W2T);

  k1_prep<<<K1_BLOCKS, 1024, 0, stream>>>(x, ea, src, dst, w1, b1, w2, Pp, Qp, offs, list, w2T);
  k2_agg<<<K2_BLOCKS, 1024, 0, stream>>>(Pp, Qp, offs, list, w2T, w1, b2, (unsigned short*)d_out);
}
```

```cpp
#include <hip/hip_runtime.h>
#include <hip/hip_bf16.h>
#include <stdint.h>

using bf16x8 = __attribute__((ext_vector_type(8))) short;
using s16x4  = __attribute__((ext_vector_type(4))) short;
using f32x4  = __attribute__((ext_vector_type(4))) float;
typedef __attribute__((address_space(3))) s16x4* lds_s16x4_ptr;

namespace {

constexpr int NN  = 10000;
constexpr int EE  = 320000;
constexpr int DD  = 128;
constexpr int HH  = 256;
constexpr int G   = 64;
constexpr int CH  = EE / G;
constexpr int OFFS_STRIDE = 10008;
constexpr int GEMM_TG = 40;
constexpr int K1_BLOCKS = G + 2 * GEMM_TG;
constexpr int W_STRIDE = 544;

constexpr size_t WS_P    = 0;
constexpr size_t WS_Q    = WS_P + (size_t)(NN + 1) * HH * 2;
constexpr size_t WS_OFFS = WS_Q + (size_t)NN * HH * 4;
constexpr size_t WS_LIST = WS_OFFS + (size_t)G * OFFS_STRIDE * 2;
constexpr size_t WS_W2T  = WS_LIST + (size_t)G * CH * 4;
constexpr size_t WS_END  = WS_W2T + (size_t)DD * HH * 2;

constexpr float NEG_LOG2E = -1.4426950408889634f;
constexpr float NEG_LN2   = -0.6931471805599453f;
constexpr unsigned NULL_REC = (unsigned)NN;
constexpr unsigned short NULL_VAL = 0x7149;

__device__ __forceinline__ float bf2f(unsigned short u) { return __uint_as_float(((unsigned)u) << 16); }
__device__ __forceinline__ unsigned short f2bf(float f) {
  __hip_bfloat16 h = __float2bfloat16(f);
  return *reinterpret_cast<unsigned short*>(&h);
}
__device__ __forceinline__ unsigned pack2(float a, float b) {
  return (unsigned)f2bf(a) | ((unsigned)f2bf(b) << 16);
}

constexpr int K1_LDS = 70400;

__global__ __launch_bounds__(1024) void k1_prep(
    const unsigned short* __restrict__ x, const unsigned short* __restrict__ eattr,
    const int* __restrict__ src, const int* __restrict__ dst,
    const unsigned short* __restrict__ w1, const unsigned short* __restrict__ b1,
    const unsigned short* __restrict__ w2,
    unsigned short* __restrict__ Pp, float* __restrict__ Qp,
    unsigned short* __restrict__ offs, unsigned* __restrict__ list,
    unsigned short* __restrict__ w2T) {
  __shared__ __attribute__((aligned(16))) unsigned char smem[K1_LDS];
  const int tid = threadIdx.x;
  const int lane = tid & 63;
  const int wave = tid >> 6;
  const int bid = blockIdx.x;

  if (bid < G) {
    unsigned* hist = reinterpret_cast<unsigned*>(smem);
    unsigned short* lst = reinterpret_cast<unsigned short*>(smem + 40064);
    unsigned* lrec = reinterpret_cast<unsigned*>(smem + 50064);
    unsigned* wsum = reinterpret_cast<unsigned*>(smem + 70064);
    const int ebase = bid * CH;
    int d[5];
    unsigned r[5], rec[5];
#pragma unroll
    for (int i = 0; i < 5; ++i) {
      const int idx = i * 1024 + tid;
      d[i] = 0; rec[i] = 0;
      if (idx < CH) {
        d[i] = dst[ebase + idx];
        rec[i] = ((unsigned)src[ebase + idx] & 0xffffu) | ((unsigned)eattr[ebase + idx] << 16);
      }
    }
    for (int i = tid; i < NN + 2; i += 1024) hist[i] = 0u;
    __syncthreads();
#pragma unroll
    for (int i = 0; i < 5; ++i) {
      const int idx = i * 1024 + tid;
      r[i] = 0;
      if (idx < CH) r[i] = atomicAdd(&hist[d[i]], 1u);
    }
    __syncthreads();
    unsigned c[10];
    unsigned sum = 0;
    if (tid < NN / 10) {
#pragma unroll
      for (int k = 0; k < 10; ++k) { c[k] = hist[tid * 10 + k]; sum += c[k]; }
    }
    unsigned incl = sum;
#pragma unroll
    for (int o = 1; o < 64; o <<= 1) {
      unsigned t = __shfl_up(incl, o, 64);
      if (lane >= o) incl += t;
    }
    if (lane == 63) wsum[wave] = incl;
    __syncthreads();
    unsigned wpre = 0;
    for (int w = 0; w < wave; ++w) wpre += wsum[w];
    unsigned excl = wpre + incl - sum;
    if (tid < NN / 10) {
#pragma unroll
      for (int k = 0; k < 10; ++k) { hist[tid * 10 + k] = excl; excl += c[k]; }
    }
    if (tid == 0) { hist[NN] = CH; hist[NN + 1] = CH; }
    __syncthreads();
#pragma unroll
    for (int i = 0; i < 5; ++i) {
      const int idx = i * 1024 + tid;
      if (idx < CH) lst[hist[d[i]] + r[i]] = (unsigned short)idx;
    }
    {
      unsigned* og = reinterpret_cast<unsigned*>(offs + (size_t)bid * OFFS_STRIDE);
      for (int i = tid; i < (NN + 2) / 2; i += 1024) og[i] = hist[2 * i] | (hist[2 * i + 1] << 16);
    }
    __syncthreads();
#pragma unroll
    for (int i = 0; i < 5; ++i) {
      const int idx = i * 1024 + tid;
      if (idx < CH) {
        const unsigned s = hist[d[i]], e = hist[d[i] + 1];
        unsigned rank = 0;
        for (unsigned j = s; j < e; ++j) rank += (lst[j] < (unsigned short)idx) ? 1u : 0u;
        lrec[s + rank] = rec[i];
      }
    }
    __syncthreads();
    for (int i = tid; i < CH / 4; i += 1024)
      reinterpret_cast<uint4*>(list + ebase)[i] = reinterpret_cast<const uint4*>(lrec)[i];
  } else {
    const int gb = bid - G;
    const int half = gb / GEMM_TG;
    const int tg = gb % GEMM_TG;
#pragma unroll
    for (int i = 0; i < 4; ++i) {
      const int k = (tid >> 5) + 32 * i;
      const int c8 = (tid & 31) * 8;
      const uint4 v = *reinterpret_cast<const uint4*>(w1 + (size_t)(half * DD + k) * HH + c8);
      *reinterpret_cast<uint4*>(smem + k * W_STRIDE + c8 * 2) = v;
    }
    if (gb < 64 && tid < 512) {
      const int i = gb * 512 + tid;
      const int k = i >> 7, oc = i & 127;
      w2T[oc * HH + k] = w2[i];
    }
    if (gb == 64 && tid < HH) Pp[(size_t)NN * HH + tid] = NULL_VAL;
    const int nt = tg * 16 + wave;
    const int ntc = nt < NN / 16 ? nt : NN / 16 - 1;
    const int node = ntc * 16 + (lane & 15);
    bf16x8 xb[4];
#pragma unroll
    for (int ks = 0; ks < 4; ++ks)
      xb[ks] = *reinterpret_cast<const bf16x8*>(x + (size_t)node * DD + ks * 32 + (lane >> 4) * 8);
    __syncthreads();
    if (nt < NN / 16) {
      const unsigned lbase = (unsigned)(size_t)smem + ((lane >> 4) * 8 + ((lane & 15) >> 2)) * W_STRIDE + (lane & 3) * 8;
      for (int ct = 0; ct < 16; ++ct) {
        f32x4 acc = {0.f, 0.f, 0.f, 0.f};
#pragma unroll
        for (int ks = 0; ks < 4; ++ks) {
          const unsigned a0 = lbase + (ks * 32) * W_STRIDE + ct * 32;
          const s16x4 lo = __builtin_amdgcn_ds_read_tr16_b64_v4i16((lds_s16x4_ptr)(size_t)a0);
          const s16x4 hi = __builtin_amdgcn_ds_read_tr16_b64_v4i16((lds_s16x4_ptr)(size_t)(a0 + 4 * W_STRIDE));
          const bf16x8 a = {lo[0], lo[1], lo[2], lo[3], hi[0], hi[1], hi[2], hi[3]};
          acc = __builtin_amdgcn_mfma_f32_16x16x32_bf16(a, xb[ks], acc, 0, 0, 0);
        }
        const int hc = ct * 16 + (lane >> 4) * 4;
        if (half == 0) {
          uint2 o;
          o.x = pack2(acc[0] * NEG_LOG2E, acc[1] * NEG_LOG2E);
          o.y = pack2(acc[2] * NEG_LOG2E, acc[3] * NEG_LOG2E);
          *reinterpret_cast<uint2*>(Pp + (size_t)node * HH + hc) = o;
        } else {
          float4 o;
          o.x = (acc[0] + bf2f(b1[hc + 0])) * NEG_LOG2E;
          o.y = (acc[1] + bf2f(b1[hc + 1])) * NEG_LOG2E;
          o.z = (acc[2] + bf2f(b1[hc + 2])) * NEG_LOG2E;
          o.w = (acc[3] + bf2f(b1[hc + 3])) * NEG_LOG2E;
          *reinterpret_cast<float4*>(Qp + (size_t)node * HH + hc) = o;
        }
      }
    }
  }
}

constexpr int K2_BLOCKS = 256;
constexpr int K2_MAXN = 48;
constexpr int QS_STRIDE = 1040;
constexpr int LCAP = 6144;
constexpr int K2_QS = 0;
constexpr int K2_L = K2_MAXN * QS_STRIDE;
constexpr int K2_DEG = K2_L + (LCAP + 16) * 4;
constexpr int K2_RP = K2_DEG + 64 * 4;
constexpr int K2_LDS = K2_RP + 64 * 4;

__device__ __forceinline__ float silu_t(float t) {
  const float ex = __builtin_amdgcn_exp2f(t);
  return __builtin_amdgcn_rcpf(1.0f + ex);
}

struct Acc { float a0, a1, a2, a3; };

__device__ __forceinline__ void edge_acc(Acc& a, unsigned rec, uint2 v, const float4& q,
                                         float wq0, float wq1, float wq2, float wq3) {
  const float e = __uint_as_float(rec & 0xffff0000u);
  const float t0 = fmaf(e, wq0, q.x) + __uint_as_float(v.x << 16);
  const float t1 = fmaf(e, wq1, q.y) + __uint_as_float(v.x & 0xffff0000u);
  const float t2 = fmaf(e, wq2, q.z) + __uint_as_float(v.y << 16);
  const float t3 = fmaf(e, wq3, q.w) + __uint_as_float(v.y & 0xffff0000u);
  a.a0 = fmaf(t0, silu_t(t0), a.a0);
  a.a1 = fmaf(t1, silu_t(t1), a.a1);
  a.a2 = fmaf(t2, silu_t(t2), a.a2);
  a.a3 = fmaf(t3, silu_t(t3), a.a3);
}

__device__ __forceinline__ void store_s(unsigned char* row, int lane, const Acc& a) {
  const float s0 = a.a0 * NEG_LN2, s1 = a.a1 * NEG_LN2, s2 = a.a2 * NEG_LN2, s3 = a.a3 * NEG_LN2;
  const unsigned short h0 = f2bf(s0), h1 = f2bf(s1), h2 = f2bf(s2), h3 = f2bf(s3);
  uint2 hv, lv;
  hv.x = (unsigned)h0 | ((unsigned)h1 << 16);
  hv.y = (unsigned)h2 | ((unsigned)h3 << 16);
  lv.x = pack2(s0 - bf2f(h0), s1 - bf2f(h1));
  lv.y = pack2(s2 - bf2f(h2), s3 - bf2f(h3));
  *reinterpret_cast<uint2*>(row + lane * 8) = hv;
  *reinterpret_cast<uint2*>(row + 512 + lane * 8) = lv;
}

__device__ __forceinline__ int wave_incl_scan(int v, int lane) {
#pragma unroll
  for (int o = 1; o < 64; o <<= 1) {
    const int t = __shfl_up(v, o, 64);
    if (lane >= o) v += t;
  }
  return v;
}

struct Grp { unsigned r0, r1, r2, r3; uint2 v0, v1, v2, v3; };

__global__ __launch_bounds__(1024) void k2_agg(
    const unsigned short* __restrict__ Pp, const float* __restrict__ Qp,
    const unsigned short* __restrict__ offs, const unsigned* __restrict__ list,
    const unsigned short* __restrict__ w2T, const unsigned short* __restrict__ w1,
    const unsigned short* __restrict__ b2, unsigned short* __restrict__ out) {
  __shared__ __attribute__((aligned(16))) unsigned char smem[K2_LDS];
  unsigned char* QS = smem + K2_QS;
  unsigned* L = reinterpret_cast<unsigned*>(smem + K2_L);
  int* degs = reinterpret_cast<int*>(smem + K2_DEG);
  int* rps = reinterpret_cast<int*>(smem + K2_RP);

  const int tid = threadIdx.x;
  const int lane = tid & 63;
  const int wave = __builtin_amdgcn_readfirstlane(tid >> 6);
  const int n0 = (blockIdx.x * NN) >> 8;
  const int n1 = ((blockIdx.x + 1) * NN) >> 8;
  const int nn = n1 - n0;

  float wq0, wq1, wq2, wq3;
  {
    const uint2 wv = *reinterpret_cast<const uint2*>(w1 + (size_t)(2 * DD) * HH + lane * 4);
    wq0 = __uint_as_float(wv.x << 16) * NEG_LOG2E;
    wq1 = __uint_as_float(wv.x & 0xffff0000u) * NEG_LOG2E;
    wq2 = __uint_as_float(wv.y << 16) * NEG_LOG2E;
    wq3 = __uint_as_float(wv.y & 0xffff0000u) * NEG_LOG2E;
  }

  int o0s[3], cnts[3], starts[3];
#pragma unroll
  for (int s = 0; s < 3; ++s) {
    const int nl = wave + 16 * s;
    o0s[s] = 0; cnts[s] = 0; starts[s] = 0;
    if (nl < nn) {
      const int n = n0 + nl;
      const int o0 = offs[lane * OFFS_STRIDE + n];
      const int o1 = offs[lane * OFFS_STRIDE + n + 1];
      const int cnt = o1 - o0;
      const int incl = wave_incl_scan(cnt, lane);
      o0s[s] = o0; cnts[s] = cnt; starts[s] = incl - cnt;
      degs[nl] = __builtin_amdgcn_readlane(incl, 63);
      *reinterpret_cast<float4*>(QS + nl * QS_STRIDE + lane * 16) =
          *reinterpret_cast<const float4*>(Qp + (size_t)n * HH + lane * 4);
    }
  }
  __syncthreads();

  const int mydeg = lane < nn ? degs[lane] : 0;
  const int mypad = (mydeg + 3) & ~3;
  const int pincl = wave_incl_scan(mypad, lane);
  const int rp = pincl - mypad;
  const int T = __builtin_amdgcn_readlane(pincl, 63);

  if (T <= LCAP) {
#pragma unroll
    for (int s = 0; s < 3; ++s) {
      const int nl = wave + 16 * s;
      if (nl < nn) {
        const int base = __builtin_amdgcn_readlane(rp, nl);
        const int dg = __builtin_amdgcn_readlane(mydeg, nl);
        for (int j = 0; j < cnts[s]; ++j) L[base + starts[s] + j] = list[lane * CH + o0s[s] + j];
        if (lane < ((dg + 3) & ~3) - dg) L[base + dg + lane] = NULL_REC;
      }
    }
    if (wave == 0 && lane < 16) L[T + lane] = NULL_REC;
    if (wave == 1) rps[lane] = rp;
    __syncthreads();

    const int owner = T > 0 ? min((rp * 16) / T, 15) : 0;
    const unsigned long long mlt = __ballot(lane < nn && owner < wave);
    const unsigned long long mle = __ballot(lane < nn && owner <= wave);
    int ni = __builtin_popcountll(mlt);
    const int nb = __builtin_popcountll(mle);
    if (ni < nb) {
      const int G0 = __builtin_amdgcn_readlane(rp, ni) >> 2;
      const int G1 = (nb < nn ? __builtin_amdgcn_readlane(rp, nb) : T) >> 2;
      int endg = (ni + 1 < nn ? __builtin_amdgcn_readlane(rp, ni + 1) : T) >> 2;
      float4 q = *reinterpret_cast<const float4*>(QS + ni * QS_STRIDE + lane * 16);
      Acc acc = {0.f, 0.f, 0.f, 0.f};
      const unsigned short* Pl = Pp + lane * 4;

      auto issue = [&](Grp& b, int g) {
        const uint4 rv = *reinterpret_cast<const uint4*>(L + g * 4);
        b.r0 = __builtin_amdgcn_readfirstlane(rv.x);
        b.r1 = __builtin_amdgcn_readfirstlane(rv.y);
        b.r2 = __builtin_amdgcn_readfirstlane(rv.z);
        b.r3 = __builtin_amdgcn_readfirstlane(rv.w);
        b.v0 = *reinterpret_cast<const uint2*>(Pl + (size_t)(b.r0 & 0xffffu) * HH);
        b.v1 = *reinterpret_cast<const uint2*>(Pl + (size_t)(b.r1 & 0xffffu) * HH);
        b.v2 = *reinterpret_cast<const uint2*>(Pl + (size_t)(b.r2 & 0xffffu) * HH);
        b.v3 = *reinterpret_cast<const uint2*>(Pl + (size_t)(b.r3 & 0xffffu) * HH);
      };
      auto compute = [&](const Grp& b, int g) {
        while (g == endg) {
          store_s(QS + ni * QS_STRIDE, lane, acc);
          acc.a0 = 0.f; acc.a1 = 0.f; acc.a2 = 0.f; acc.a3 = 0.f;
          ++ni;
          endg = (ni + 1 < nn ? rps[ni + 1] : T) >> 2;
          endg = __builtin_amdgcn_readfirstlane(endg);
          q = *reinterpret_cast<const float4*>(QS + ni * QS_STRIDE + lane * 16);
        }
        edge_acc(acc, b.r0, b.v0, q, wq0, wq1, wq2, wq3);
        edge_acc(acc, b.r1, b.v1, q, wq0, wq1, wq2, wq3);
        edge_acc(acc, b.r2, b.v2, q, wq0, wq1, wq2, wq3);
        edge_acc(acc, b.r3, b.v3, q, wq0, wq1, wq2, wq3);
      };

      Grp A, B, C;
      int g = G0;
      if (g < G1) {
        issue(A, g);
        issue(B, g + 1);
        for (; g < G1; g += 3) {
          issue(C, g + 2); compute(A, g);
          issue(A, g + 3); if (g + 1 < G1) compute(B, g + 1);
          issue(B, g + 4); if (g + 2 < G1) compute(C, g + 2);
        }
      }
      for (; ni < nb; ++ni) {
        store_s(QS + ni * QS_STRIDE, lane, acc);
        acc.a0 = 0.f; acc.a1 = 0.f; acc.a2 = 0.f; acc.a3 = 0.f;
      }
    }
  } else {
    unsigned* mystag = L + wave * 64;
#pragma unroll
    for (int s = 0; s < 3; ++s) {
      const int nl = wave + 16 * s;
      if (nl < nn) {
        const int total = __builtin_amdgcn_readlane(mydeg, nl);
        const float4 q = *reinterpret_cast<const float4*>(QS + nl * QS_STRIDE + lane * 16);
        Acc acc = {0.f, 0.f, 0.f, 0.f};
        for (int w0 = 0; w0 < total; w0 += 64) {
          const int jlo = max(w0 - starts[s], 0);
          const int jhi = min(cnts[s], w0 + 64 - starts[s]);
          for (int j = jlo; j < jhi; ++j) mystag[starts[s] + j - w0] = list[lane * CH + o0s[s] + j];
          const int m = min(64, total - w0);
          const unsigned rec = (lane < m) ? mystag[lane] : NULL_REC;
          for (int i = 0; i < m; ++i) {
            const unsigned rr = __builtin_amdgcn_readlane(rec, i);
            const uint2 v = *reinterpret_cast<const uint2*>(Pp + (size_t)(rr & 0xffffu) * HH + lane * 4);
            edge_acc(acc, rr, v, q, wq0, wq1, wq2, wq3);
          }
        }
        store_s(QS + nl * QS_STRIDE, lane, acc);
      }
    }
  }
  __syncthreads();

  const int ntiles = (nn + 15) >> 4;
  for (int t = wave; t < ntiles * 8; t += 16) {
    const int ntile = t >> 3, oct = t & 7;
    f32x4 acc = {0.f, 0.f, 0.f, 0.f};
    const int srow = ntile * 16 + (lane & 15);
#pragma unroll
    for (int ks = 0; ks < 8; ++ks) {
      const bf16x8 a = *reinterpret_cast<const bf16x8*>(w2T + (oct * 16 + (lane & 15)) * HH + ks * 32 + (lane >> 4) * 8);
      const bf16x8 bh = *reinterpret_cast<const bf16x8*>(QS + srow * QS_STRIDE + ks * 64 + (lane >> 4) * 16);
      const bf16x8 bl = *reinterpret_cast<const bf16x8*>(QS + srow * QS_STRIDE + 512 + ks * 64 + (lane >> 4) * 16);
      acc = __builtin_amdgcn_mfma_f32_16x16x32_bf16(a, bh, acc, 0, 0, 0);
      acc = __builtin_amdgcn_mfma_f32_16x16x32_bf16(a, bl, acc, 0, 0, 0);
    }
    if (srow < nn) {
      const int oc = oct * 16 + (lane >> 4) * 4;
      const float dg = (float)degs[srow];
      uint2 o;
      o.x = pack2(acc[0] + dg * bf2f(b2[oc + 0]), acc[1] + dg * bf2f(b2[oc + 1]));
      o.y = pack2(acc[2] + dg * bf2f(b2[oc + 2]), acc[3] + dg * bf2f(b2[oc + 3]));
      *reinterpret_cast<uint2*>(out + (size_t)(n0 + srow) * DD + oc) = o;
    }
  }
}

}

extern "C" void kernel_launch(void* const* d_in, const int* in_sizes, int n_in,
                              void* d_out, int out_size, void* d_ws, size_t ws_size,
                              hipStream_t stream) {
  const unsigned short* x  = (const unsigned short*)d_in[0];
  const unsigned short* ea = (const unsigned short*)d_in[1];
  const int* src = (const int*)d_in[2];
  const int* dst = (const int*)d_in[3];
  const unsigned short* w1 = (const unsigned short*)d_in[4];
  const unsigned short* b1 = (const unsigned short*)d_in[5];
  const unsigned short* w2 = (const unsigned short*)d_in[6];
  const unsigned short* b2 = (const unsigned short*)d_in[7];
  unsigned char* ws = (unsigned char*)d_ws;
  unsigned short* Pp = (unsigned short*)(ws + WS_P);
  float* Qp = (float*)(ws + WS_Q);
  unsigned short* offs = (unsigned short*)(ws + WS_OFFS);
  unsigned* list = (unsigned*)(ws + WS_LIST);
  unsigned short* w2T = (unsigned short*)(ws + WS_W2T);

  k1_prep<<<K1_BLOCKS, 1024, 0, stream>>>(x, ea, src, dst, w1, b1, w2, Pp, Qp, offs, list, w2T);
  k2_agg<<<K2_BLOCKS, 1024, 0, stream>>>(Pp, Qp, offs, list, w2T, w1, b2, (unsigned short*)d_out);
}
```

```cpp
#include <hip/hip_runtime.h>
#include <hip/hip_bf16.h>
#include <stdint.h>

using bf16x8 = __attribute__((ext_vector_type(8))) short;
using s16x4  = __attribute__((ext_vector_type(4))) short;
using f32x4  = __attribute__((ext_vector_type(4))) float;
typedef __attribute__((address_space(3))) s16x4* lds_s16x4_ptr;

namespace {

constexpr int NN  = 10000;
constexpr int EE  = 320000;
constexpr int DD  = 128;
constexpr int HH  = 256;
constexpr int G   = 64;
constexpr int CH  = EE / G;
constexpr int NPB = 40;
constexpr int NB  = NN / NPB;
constexpr int CAP = 64;
constexpr int GEMM_BLOCKS = 160;
constexpr int K1_BLOCKS = G + GEMM_BLOCKS;
constexpr int W_STRIDE = 288;
constexpr int O_STRIDE = 272;

constexpr size_t WS_P    = 0;
constexpr size_t WS_Q    = WS_P + (size_t)(NN + 1) * HH * 2;
constexpr size_t WS_BK   = WS_Q + (size_t)NN * HH * 2;
constexpr size_t WS_CNT  = WS_BK + (size_t)NB * G * CAP * 8;
constexpr size_t WS_W2T  = WS_CNT + (size_t)NB * G * 4;
constexpr size_t WS_END  = WS_W2T + (size_t)DD * HH * 2;

constexpr float NEG_LOG2E = -1.4426950408889634f;
constexpr float NEG_LN2   = -0.6931471805599453f;
constexpr unsigned NULL_REC = (unsigned)NN;
constexpr unsigned short NULL_VAL = 0x7149;

__device__ __forceinline__ float bf2f(unsigned short u) { return __uint_as_float(((unsigned)u) << 16); }
__device__ __forceinline__ unsigned short f2bf(float f) {
  __hip_bfloat16 h = __float2bfloat16(f);
  return *reinterpret_cast<unsigned short*>(&h);
}
__device__ __forceinline__ unsigned pack2(float a, float b) {
  return (unsigned)f2bf(a) | ((unsigned)f2bf(b) << 16);
}

constexpr int K1_W = 0;
constexpr int K1_O = DD * W_STRIDE;
constexpr int K1_LDS = K1_O + 16 * 16 * O_STRIDE;

__global__ __launch_bounds__(1024) void k1_prep(
    const unsigned short* __restrict__ x, const unsigned short* __restrict__ eattr,
    const int* __restrict__ src, const int* __restrict__ dst,
    const unsigned short* __restrict__ w1, const unsigned short* __restrict__ b1,
    const unsigned short* __restrict__ w2,
    unsigned short* __restrict__ Pp, unsigned short* __restrict__ Qp,
    uint2* __restrict__ bk, unsigned* __restrict__ cntg,
    unsigned short* __restrict__ w2T) {
  __shared__ __attribute__((aligned(16))) unsigned char smem[K1_LDS];
  const int tid = threadIdx.x;
  const int lane = tid & 63;
  const int wave = tid >> 6;
  const int bid = blockIdx.x;

  if (bid < G) {
    unsigned* hist = reinterpret_cast<unsigned*>(smem);
    const int ebase = bid * CH;
    int d[5];
    unsigned rec[5];
#pragma unroll
    for (int i = 0; i < 5; ++i) {
      const int idx = i * 1024 + tid;
      d[i] = 0; rec[i] = 0;
      if (idx < CH) {
        d[i] = dst[ebase + idx];
        rec[i] = ((unsigned)src[ebase + idx] & 0xffffu) | ((unsigned)eattr[ebase + idx] << 16);
      }
    }
    if (tid < 256) hist[tid] = 0u;
    __syncthreads();
#pragma unroll
    for (int i = 0; i < 5; ++i) {
      const int idx = i * 1024 + tid;
      if (idx < CH) {
        const int bin = d[i] / NPB;
        const int nib = d[i] - bin * NPB;
        const unsigned slot = atomicAdd(&hist[bin], 1u);
        if (slot < CAP) bk[((size_t)bin * G + bid) * CAP + slot] = make_uint2(((unsigned)nib << 16) | (unsigned)idx, rec[i]);
      }
    }
    __syncthreads();
    if (tid < NB) cntg[tid * G + bid] = hist[tid];
  } else {
    const int gb = bid - G;
    const int half = gb / 80;
    const int hh = (gb / 40) & 1;
    const int tg = gb % 40;
#pragma unroll
    for (int i = 0; i < 2; ++i) {
      const int k = (tid >> 4) + 64 * i;
      const int c8 = (tid & 15) * 8;
      const uint4 v = *reinterpret_cast<const uint4*>(w1 + (size_t)(half * DD + k) * HH + hh * 128 + c8);
      *reinterpret_cast<uint4*>(smem + K1_W + k * W_STRIDE + c8 * 2) = v;
    }
    if (gb < 64 && tid < 512) {
      const int i = gb * 512 + tid;
      const int k = i >> 7, oc = i & 127;
      w2T[oc * HH + k] = w2[i];
    }
    if (gb == 64 && tid < HH) Pp[(size_t)NN * HH + tid] = NULL_VAL;
    const int nt = tg * 16 + wave;
    const int ntc = nt < NN / 16 ? nt : NN / 16 - 1;
    const int node = ntc * 16 + (lane & 15);
    bf16x8 xb[4];
#pragma unroll
    for (int ks = 0; ks < 4; ++ks)
      xb[ks] = *reinterpret_cast<const bf16x8*>(x + (size_t)node * DD + ks * 32 + (lane >> 4) * 8);
    float bias[8][4];
#pragma unroll
    for (int ct = 0; ct < 8; ++ct) {
      const uint2 bv = *reinterpret_cast<const uint2*>(b1 + hh * 128 + ct * 16 + (lane >> 4) * 4);
      const float m = half ? 1.0f : 0.0f;
      bias[ct][0] = __uint_as_float(bv.x << 16) * m;
      bias[ct][1] = __uint_as_float(bv.x & 0xffff0000u) * m;
      bias[ct][2] = __uint_as_float(bv.y << 16) * m;
      bias[ct][3] = __uint_as_float(bv.y & 0xffff0000u) * m;
    }
    __syncthreads();
    if (nt < NN / 16) {
      unsigned char* ob = smem + K1_O + wave * (16 * O_STRIDE);
      const unsigned lbase = (unsigned)(size_t)smem + K1_W + ((lane >> 4) * 8 + ((lane & 15) >> 2)) * W_STRIDE + (lane & 3) * 8;
#pragma unroll
      for (int ct = 0; ct < 8; ++ct) {
        f32x4 acc = {bias[ct][0], bias[ct][1], bias[ct][2], bias[ct][3]};
#pragma unroll
        for (int ks = 0; ks < 4; ++ks) {
          const unsigned a0 = lbase + (ks * 32) * W_STRIDE + ct * 32;
          const s16x4 lo = __builtin_amdgcn_ds_read_tr16_b64_v4i16((lds_s16x4_ptr)(size_t)a0);
          const s16x4 hi = __builtin_amdgcn_ds_read_tr16_b64_v4i16((lds_s16x4_ptr)(size_t)(a0 + 4 * W_STRIDE));
          const bf16x8 a = {lo[0], lo[1], lo[2], lo[3], hi[0], hi[1], hi[2], hi[3]};
          acc = __builtin_amdgcn_mfma_f32_16x16x32_bf16(a, xb[ks], acc, 0, 0, 0);
        }
        uint2 o;
        o.x = pack2(acc[0] * NEG_LOG2E, acc[1] * NEG_LOG2E);
        o.y = pack2(acc[2] * NEG_LOG2E, acc[3] * NEG_LOG2E);
        *reinterpret_cast<uint2*>(ob + (lane & 15) * O_STRIDE + ct * 32 + (lane >> 4) * 8) = o;
      }
      unsigned short* dstp = (half ? Qp : Pp) + (size_t)(nt * 16) * HH + hh * 128;
#pragma unroll
      for (int r = 0; r < 4; ++r) {
        const int row = r * 4 + (lane >> 4);
        const uint4 v = *reinterpret_cast<const uint4*>(ob + row * O_STRIDE + (lane & 15) * 16);
        *reinterpret_cast<uint4*>(dstp + (size_t)row * HH + (lane & 15) * 8) = v;
      }
    }
  }
}

constexpr int QS_STRIDE = 1040;
constexpr int LCAP = 6144;
constexpr int K2_QS   = 0;
constexpr int K2_Q    = K2_QS + 48 * QS_STRIDE;
constexpr int K2_CAR  = K2_Q + NPB * HH * 2;
constexpr int K2_KEY  = K2_CAR + 16 * HH * 4;
constexpr int K2_CM   = K2_KEY + G * CAP * 4;
constexpr int K2_L    = K2_CM + NPB * G * 4;
constexpr int K2_DEG  = K2_L + (LCAP + 16) * 4;
constexpr int K2_RP   = K2_DEG + 64 * 4;
constexpr int K2_CN   = K2_RP + 64 * 4;
constexpr int K2_BC   = K2_CN + 16 * 4;
constexpr int K2_FLAG = K2_BC + 64 * 4;
constexpr int K2_LDS  = K2_FLAG + 16;

__device__ __forceinline__ float sig_t(float t) {
  return __builtin_amdgcn_rcpf(1.0f + __builtin_amdgcn_exp2f(t));
}

struct Acc { float a0, a1, a2, a3; };

__device__ __forceinline__ void edge_acc(Acc& a, unsigned rec, uint2 v, const float4& q,
                                         float wq0, float wq1, float wq2, float wq3) {
  const float e = __uint_as_float(rec & 0xffff0000u);
  const float t0 = fmaf(e, wq0, q.x) + __uint_as_float(v.x << 16);
  const float t1 = fmaf(e, wq1, q.y) + __uint_as_float(v.x & 0xffff0000u);
  const float t2 = fmaf(e, wq2, q.z) + __uint_as_float(v.y << 16);
  const float t3 = fmaf(e, wq3, q.w) + __uint_as_float(v.y & 0xffff0000u);
  a.a0 = fmaf(t0, sig_t(t0), a.a0);
  a.a1 = fmaf(t1, sig_t(t1), a.a1);
  a.a2 = fmaf(t2, sig_t(t2), a.a2);
  a.a3 = fmaf(t3, sig_t(t3), a.a3);
}

__device__ __forceinline__ float4 load_q(const unsigned char* qrow, int lane) {
  const uint2 v = *reinterpret_cast<const uint2*>(qrow + lane * 8);
  return make_float4(__uint_as_float(v.x << 16), __uint_as_float(v.x & 0xffff0000u),
                     __uint_as_float(v.y << 16), __uint_as_float(v.y & 0xffff0000u));
}

__device__ __forceinline__ int wave_incl_scan(int v, int lane) {
#pragma unroll
  for (int o = 1; o < 64; o <<= 1) {
    const int t = __shfl_up(v, o, 64);
    if (lane >= o) v += t;
  }
  return v;
}

struct Grp { unsigned r0, r1, r2, r3; uint2 v0, v1, v2, v3; };

__global__ __launch_bounds__(1024) void k2_agg(
    const unsigned short* __restrict__ Pp, const unsigned short* __restrict__ Qp,
    const uint2* __restrict__ bk, const unsigned* __restrict__ cntg,
    const unsigned short* __restrict__ w2T, const unsigned short* __restrict__ w1,
    const unsigned short* __restrict__ b2,
    const unsigned short* __restrict__ eattr, const int* __restrict__ src, const int* __restrict__ dst,
    unsigned short* __restrict__ out) {
  __shared__ __attribute__((aligned(16))) unsigned char smem[K2_LDS];
  unsigned char* QS = smem + K2_QS;
  unsigned char* Qs = smem + K2_Q;
  float* car = reinterpret_cast<float*>(smem + K2_CAR);
  unsigned* keys = reinterpret_cast<unsigned*>(smem + K2_KEY);
  unsigned* cm = reinterpret_cast<unsigned*>(smem + K2_CM);
  unsigned* L = reinterpret_cast<unsigned*>(smem + K2_L);
  int* degs = reinterpret_cast<int*>(smem + K2_DEG);
  int* rps = reinterpret_cast<int*>(smem + K2_RP);
  int* cnode = reinterpret_cast<int*>(smem + K2_CN);
  unsigned* bcnt = reinterpret_cast<unsigned*>(smem + K2_BC);
  int* flag = reinterpret_cast<int*>(smem + K2_FLAG);

  const int tid = threadIdx.x;
  const int lane = tid & 63;
  const int wave = __builtin_amdgcn_readfirstlane(tid >> 6);
  const int n0 = blockIdx.x * NPB;

  const int mych = tid >> 4;
  const int mysl = (tid & 15) * 4;
  const unsigned mycnt = cntg[blockIdx.x * G + mych];
  uint4 ra, rb;
  {
    const uint4* bp = reinterpret_cast<const uint4*>(bk + ((size_t)blockIdx.x * G + mych) * CAP + mysl);
    ra = bp[0]; rb = bp[1];
  }
  {
    const uint4* qg = reinterpret_cast<const uint4*>(Qp + (size_t)n0 * HH);
    for (int i = tid; i < NPB * HH / 8; i += 1024) reinterpret_cast<uint4*>(Qs)[i] = qg[i];
  }
  float wq0, wq1, wq2, wq3;
  {
    const uint2 wv = *reinterpret_cast<const uint2*>(w1 + (size_t)(2 * DD) * HH + lane * 4);
    wq0 = __uint_as_float(wv.x << 16) * NEG_LOG2E;
    wq1 = __uint_as_float(wv.x & 0xffff0000u) * NEG_LOG2E;
    wq2 = __uint_as_float(wv.y << 16) * NEG_LOG2E;
    wq3 = __uint_as_float(wv.y & 0xffff0000u) * NEG_LOG2E;
  }
  for (int i = tid; i < NPB * G; i += 1024) cm[i] = 0u;
  if (tid == 0) *flag = 0;
  if (tid < 16) cnode[tid] = -1;
  __syncthreads();

  const unsigned rk[4] = {ra.x, ra.z, rb.x, rb.z};
  const unsigned rv[4] = {ra.y, ra.w, rb.y, rb.w};
  if (mycnt > (unsigned)CAP) *flag = 1;
  if ((tid & 15) == 0) bcnt[mych] = mycnt;
  *reinterpret_cast<uint4*>(keys + mych * CAP + mysl) = make_uint4(rk[0], rk[1], rk[2], rk[3]);
#pragma unroll
  for (int j = 0; j < 4; ++j)
    if ((unsigned)(mysl + j) < mycnt && mycnt <= (unsigned)CAP) atomicAdd(&cm[(rk[j] >> 16) * G + mych], 1u);
  __syncthreads();

#pragma unroll
  for (int s = 0; s < 3; ++s) {
    const int nl = wave + 16 * s;
    if (nl < NPB) {
      const int c = (int)cm[nl * G + lane];
      const int incl = wave_incl_scan(c, lane);
      cm[nl * G + lane] = (unsigned)(incl - c);
      degs[nl] = __builtin_amdgcn_readlane(incl, 63);
    }
  }
  __syncthreads();

  const int mydeg = lane < NPB ? degs[lane] : 0;
  const int mypad = (mydeg + 3) & ~3;
  const int pincl = wave_incl_scan(mypad, lane);
  const int rp = pincl - mypad;
  const int T = __builtin_amdgcn_readlane(pincl, 63);
  const bool fast = (T <= LCAP) && (__builtin_amdgcn_readfirstlane(*flag) == 0);

  if (fast) {
    if (wave == 1) rps[lane] = rp;
    __syncthreads();
#pragma unroll
    for (int j = 0; j < 4; ++j) {
      if ((unsigned)(mysl + j) < mycnt) {
        const unsigned key = rk[j];
        const unsigned nib = key >> 16;
        unsigned rank = 0;
        for (unsigned s = 0; s < mycnt; ++s) {
          const unsigned k2 = keys[mych * CAP + s];
          rank += ((k2 >> 16) == nib && k2 < key) ? 1u : 0u;
        }
        L[rps[nib] + cm[nib * G + mych] + rank] = rv[j];
      }
    }
#pragma unroll
    for (int s = 0; s < 3; ++s) {
      const int nl = wave + 16 * s;
      if (nl < NPB) {
        const int base = __builtin_amdgcn_readlane(rp, nl);
        const int dg = __builtin_amdgcn_readlane(mydeg, nl);
        if (lane < ((dg + 3) & ~3) - dg) L[base + dg + lane] = NULL_REC;
        if (dg == 0) *reinterpret_cast<float4*>(QS + nl * QS_STRIDE + lane * 16) = make_float4(0.f, 0.f, 0.f, 0.f);
      }
    }
    if (wave == 0 && lane < 16) L[T + lane] = NULL_REC;
    __syncthreads();

    const int TG = T >> 2;
    const int G0 = (wave * TG) >> 4;
    const int G1 = ((wave + 1) * TG) >> 4;
    if (G0 < G1) {
      const int sg = rp >> 2;
      int ni = __builtin_popcountll(__ballot(lane < NPB && sg <= G0)) - 1;
      bool mid = __builtin_amdgcn_readlane(sg, ni) < G0;
      int endg = (ni + 1 < NPB ? __builtin_amdgcn_readlane(rp, ni + 1) : T) >> 2;
      float4 q = load_q(Qs + ni * (HH * 2), lane);
      Acc acc = {0.f, 0.f, 0.f, 0.f};
      const unsigned short* Pl = Pp + lane * 4;

      auto flush = [&]() {
        float* dstrow = mid ? (car + wave * HH) : reinterpret_cast<float*>(QS + ni * QS_STRIDE);
        *reinterpret_cast<float4*>(dstrow + lane * 4) = make_float4(acc.a0, acc.a1, acc.a2, acc.a3);
        if (mid) cnode[wave] = ni;
        mid = false;
        acc.a0 = 0.f; acc.a1 = 0.f; acc.a2 = 0.f; acc.a3 = 0.f;
      };
      auto issue = [&](Grp& b, int g) {
        const uint4 r4 = *reinterpret_cast<const uint4*>(L + g * 4);
        b.r0 = __builtin_amdgcn_readfirstlane(r4.x);
        b.r1 = __builtin_amdgcn_readfirstlane(r4.y);
        b.r2 = __builtin_amdgcn_readfirstlane(r4.z);
        b.r3 = __builtin_amdgcn_readfirstlane(r4.w);
        b.v0 = *reinterpret_cast<const uint2*>(Pl + (size_t)(b.r0 & 0xffffu) * HH);
        b.v1 = *reinterpret_cast<const uint2*>(Pl + (size_t)(b.r1 & 0xffffu) * HH);
        b.v2 = *reinterpret_cast<const uint2*>(Pl + (size_t)(b.r2 & 0xffffu) * HH);
        b.v3 = *reinterpret_cast<const uint2*>(Pl + (size_t)(b.r3 & 0xffffu) * HH);
      };
      auto compute = [&](const Grp& b, int g) {
        if (g == endg) {
          flush();
          do {
            ++ni;
            endg = __builtin_amdgcn_readfirstlane((ni + 1 < NPB ? rps[ni + 1] : T) >> 2);
          } while (g == endg);
          q = load_q(Qs + ni * (HH * 2), lane);
        }
        edge_acc(acc, b.r0, b.v0, q, wq0, wq1, wq2, wq3);
        edge_acc(acc, b.r1, b.v1, q, wq0, wq1, wq2, wq3);
        edge_acc(acc, b.r2, b.v2, q, wq0, wq1, wq2, wq3);
        edge_acc(acc, b.r3, b.v3, q, wq0, wq1, wq2, wq3);
      };

      Grp A, B, C;
      issue(A, G0);
      issue(B, G0 + 1);
      for (int g = G0; g < G1; g += 3) {
        issue(C, g + 2); compute(A, g);
        issue(A, g + 3); if (g + 1 < G1) compute(B, g + 1);
        issue(B, g + 4); if (g + 2 < G1) compute(C, g + 2);
      }
      flush();
    }
  } else {
#pragma unroll 1
    for (int s = 0; s < 3; ++s) {
      const int nl = wave + 16 * s;
      if (nl < NPB) {
        const float4 q = load_q(Qs + nl * (HH * 2), lane);
        Acc acc = {0.f, 0.f, 0.f, 0.f};
        int total = 0;
        for (int e0 = 0; e0 < EE; e0 += 64) {
          const bool hit = dst[e0 + lane] == n0 + nl;
          unsigned long long m = __ballot(hit);
          if (m != 0ull) {
            const unsigned rec = hit ? (((unsigned)src[e0 + lane] & 0xffffu) | ((unsigned)eattr[e0 + lane] << 16)) : NULL_REC;
            total += __builtin_popcountll(m);
            while (m != 0ull) {
              const int i = __builtin_ctzll(m);
              m &= m - 1ull;
              const unsigned rr = __builtin_amdgcn_readlane(rec, i);
              const uint2 v = *reinterpret_cast<const uint2*>(Pp + (size_t)(rr & 0xffffu) * HH + lane * 4);
              edge_acc(acc, rr, v, q, wq0, wq1, wq2, wq3);
            }
          }
        }
        *reinterpret_cast<float4*>(QS + nl * QS_STRIDE + lane * 16) = make_float4(acc.a0, acc.a1, acc.a2, acc.a3);
        degs[nl] = total;
      }
    }
  }

  const int oct = wave & 7;
  bf16x8 wf[8];
#pragma unroll
  for (int ks = 0; ks < 8; ++ks)
    wf[ks] = *reinterpret_cast<const bf16x8*>(w2T + (oct * 16 + (lane & 15)) * HH + ks * 32 + (lane >> 4) * 8);
  __syncthreads();

#pragma unroll
  for (int s = 0; s < 3; ++s) {
    const int nl = wave + 16 * s;
    if (nl < NPB) {
      unsigned char* row = QS + nl * QS_STRIDE;
      float4 v = *reinterpret_cast<const float4*>(row + lane * 16);
      for (int w = 0; w < 16; ++w) {
        if (cnode[w] == nl) {
          const float4 c = *reinterpret_cast<const float4*>(car + w * HH + lane * 4);
          v.x += c.x; v.y += c.y; v.z += c.z; v.w += c.w;
        }
      }
      const float s0 = v.x * NEG_LN2, s1 = v.y * NEG_LN2, s2 = v.z * NEG_LN2, s3 = v.w * NEG_LN2;
      const unsigned short h0 = f2bf(s0), h1 = f2bf(s1), h2 = f2bf(s2), h3 = f2bf(s3);
      uint2 hv, lv;
      hv.x = (unsigned)h0 | ((unsigned)h1 << 16);
      hv.y = (unsigned)h2 | ((unsigned)h3 << 16);
      lv.x = pack2(s0 - bf2f(h0), s1 - bf2f(h1));
      lv.y = pack2(s2 - bf2f(h2), s3 - bf2f(h3));
      __builtin_amdgcn_wave_barrier();
      *reinterpret_cast<uint2*>(row + lane * 8) = hv;
      *reinterpret_cast<uint2*>(row + 512 + lane * 8) = lv;
    }
  }
  __syncthreads();

  for (int ntile = wave >> 3; ntile < 3; ntile += 2) {
    f32x4 acc = {0.f, 0.f, 0.f, 0.f};
    const int srow = ntile * 16 + (lane & 15);
#pragma unroll
    for (int ks = 0; ks < 8; ++ks) {
      const bf16x8 bh = *reinterpret_cast<const bf16x8*>(QS + srow * QS_STRIDE + ks * 64 + (lane >> 4) * 16);
      const bf16x8 bl = *reinterpret_cast<const bf16x8*>(QS + srow * QS_STRIDE + 512 + ks * 64 + (lane >> 4) * 16);
      acc = __builtin_amdgcn_mfma_f32_16x16x32_bf16(wf[ks], bh, acc, 0, 0, 0);
      acc = __builtin_amdgcn_mfma_f32_16x16x32_bf16(wf[ks], bl, acc, 0, 0, 0);
    }
    if (srow < NPB) {
      const int oc = oct * 16 + (lane >> 4) * 4;
      const float dg = (float)degs[srow];
      uint2 o;
      o.x = pack2(acc[0] + dg * bf2f(b2[oc + 0]), acc[1] + dg * bf2f(b2[oc + 1]));
      o.y = pack2(acc[2] + dg * bf2f(b2[oc + 2]), acc[3] + dg * bf2f(b2[oc + 3]));
      *reinterpret_cast<uint2*>(out + (size_t)(n0 + srow) * DD + oc) = o;
    }
  }
}

}

extern "C" void kernel_launch(void* const* d_in, const int* in_sizes, int n_in,
                              void* d_out, int out_size, void* d_ws, size_t ws_size,
                              hipStream_t stream) {
  const unsigned short* x  = (const unsigned short*)d_in[0];
  const unsigned short* ea = (const unsigned short*)d_in[1];
  const int* src = (const int*)d_in[2];
  const int* dst = (const int*)d_in[3];
  const unsigned short* w1 = (const unsigned short*)d_in[4];
  const unsigned short* b1 = (const unsigned short*)d_in[5];
  const unsigned short* w2 = (const unsigned short*)d_in[6];
  const unsigned short* b2 = (const unsigned short*)d_in[7];
  unsigned char* ws = (unsigned char*)d_ws;
  unsigned short* Pp = (unsigned short*)(ws + WS_P);
  unsigned short* Qp = (unsigned short*)(ws + WS_Q);
  uint2* bk = (uint2*)(ws + WS_BK);
  unsigned* cntg = (unsigned*)(ws + WS_CNT);
  unsigned short* w2T = (unsigned short*)(ws + WS_W2T);

  k1_prep<<<K1_BLOCKS, 1024, 0, stream>>>(x, ea, src, dst, w1, b1, w2, Pp, Qp, bk, cntg, w2T);
  k2_agg<<<NB, 1024, 0, stream>>>(Pp, Qp, bk, cntg, w2T, w1, b2, ea, src, dst, (unsigned short*)d_out);
}
```

```cpp
#include <hip/hip_runtime.h>
#include <hip/hip_bf16.h>
#include <stdint.h>

using bf16x8 = __attribute__((ext_vector_type(8))) short;
using s16x4  = __attribute__((ext_vector_type(4))) short;
using f32x4  = __attribute__((ext_vector_type(4))) float;
typedef __attribute__((address_space(3))) s16x4* lds_s16x4_ptr;

namespace {

constexpr int NN  = 10000;
constexpr int EE  = 320000;
constexpr int DD  = 128;
constexpr int HH  = 256;
constexpr int G   = 64;
constexpr int CH  = EE / G;
constexpr int NPB = 40;
constexpr int NB  = NN / NPB;
constexpr int CAP = 64;
constexpr int GEMM_BLOCKS = 160;
constexpr int K1_BLOCKS = G + GEMM_BLOCKS;
constexpr int W_STRIDE = 288;
constexpr int O_STRIDE = 272;

constexpr size_t WS_P    = 0;
constexpr size_t WS_Q    = WS_P + (size_t)(NN + 1) * HH * 2;
constexpr size_t WS_BK   = WS_Q + (size_t)NN * HH * 2;
constexpr size_t WS_CNT  = WS_BK + (size_t)NB * G * CAP * 8;
constexpr size_t WS_W2T  = WS_CNT + (size_t)NB * G * 4;
constexpr size_t WS_END  = WS_W2T + (size_t)DD * HH * 2;

constexpr float NEG_LOG2E = -1.4426950408889634f;
constexpr float NEG_LN2   = -0.6931471805599453f;
constexpr unsigned NULL_REC = (unsigned)NN;
constexpr unsigned short NULL_VAL = 0x7149;

__device__ __forceinline__ float bf2f(unsigned short u) { return __uint_as_float(((unsigned)u) << 16); }
__device__ __forceinline__ unsigned short f2bf(float f) {
  __hip_bfloat16 h = __float2bfloat16(f);
  return *reinterpret_cast<unsigned short*>(&h);
}
__device__ __forceinline__ unsigned pack2(float a, float b) {
  return (unsigned)f2bf(a) | ((unsigned)f2bf(b) << 16);
}

constexpr int K1_W = 0;
constexpr int K1_O = DD * W_STRIDE;
constexpr int K1_LDS = K1_O + 8 * 16 * O_STRIDE;

__global__ __launch_bounds__(1024) void k1_prep(
    const unsigned short* __restrict__ x, const unsigned short* __restrict__ eattr,
    const int* __restrict__ src, const int* __restrict__ dst,
    const unsigned short* __restrict__ w1, const unsigned short* __restrict__ b1,
    const unsigned short* __restrict__ w2,
    unsigned short* __restrict__ Pp, unsigned short* __restrict__ Qp,
    uint2* __restrict__ bk, unsigned* __restrict__ cntg,
    unsigned short* __restrict__ w2T) {
  __shared__ __attribute__((aligned(16))) unsigned char smem[K1_LDS];
  const int tid = threadIdx.x;
  const int lane = tid & 63;
  const int wave = tid >> 6;
  const int bid = blockIdx.x;

  if (bid < G) {
    unsigned* hist = reinterpret_cast<unsigned*>(smem);
    const int ebase = bid * CH;
    int d[5];
    unsigned rec[5];
#pragma unroll
    for (int i = 0; i < 5; ++i) {
      const int idx = i * 1024 + tid;
      d[i] = 0; rec[i] = 0;
      if (idx < CH) {
        d[i] = dst[ebase + idx];
        rec[i] = ((unsigned)src[ebase + idx] & 0xffffu) | ((unsigned)eattr[ebase + idx] << 16);
      }
    }
    if (tid < 256) hist[tid] = 0u;
    __syncthreads();
#pragma unroll
    for (int i = 0; i < 5; ++i) {
      const int idx = i * 1024 + tid;
      if (idx < CH) {
        const int bin = d[i] / NPB;
        const int nib = d[i] - bin * NPB;
        const unsigned slot = atomicAdd(&hist[bin], 1u);
        if (slot < CAP) bk[((size_t)bin * G + bid) * CAP + slot] = make_uint2(((unsigned)nib << 16) | (unsigned)idx, rec[i]);
      }
    }
    __syncthreads();
    if (tid < NB) cntg[tid * G + bid] = hist[tid];
  } else {
    const int gb = bid - G;
    const int half = gb / 80;
    const int hh = (gb / 40) & 1;
    const int tg = gb % 40;
#pragma unroll
    for (int i = 0; i < 2; ++i) {
      const int k = (tid >> 4) + 64 * i;
      const int c8 = (tid & 15) * 8;
      const uint4 v = *reinterpret_cast<const uint4*>(w1 + (size_t)(half * DD + k) * HH + hh * 128 + c8);
      *reinterpret_cast<uint4*>(smem + K1_W + k * W_STRIDE + c8 * 2) = v;
    }
    if (gb < 64 && tid < 512) {
      const int i = gb * 512 + tid;
      const int k = i >> 7, oc = i & 127;
      w2T[oc * HH + k] = w2[i];
    }
    if (gb == 64 && tid < HH) Pp[(size_t)NN * HH + tid] = NULL_VAL;
    const int nt0 = tg * 16 + wave * 2;
    const bool active = wave < 8 && nt0 < NN / 16;
    const int ntc0 = nt0 < NN / 16 ? nt0 : NN / 16 - 1;
    const int ntc1 = nt0 + 1 < NN / 16 ? nt0 + 1 : NN / 16 - 1;
    bf16x8 xb0[4], xb1[4];
#pragma unroll
    for (int ks = 0; ks < 4; ++ks) {
      xb0[ks] = *reinterpret_cast<const bf16x8*>(x + (size_t)(ntc0 * 16 + (lane & 15)) * DD + ks * 32 + (lane >> 4) * 8);
      xb1[ks] = *reinterpret_cast<const bf16x8*>(x + (size_t)(ntc1 * 16 + (lane & 15)) * DD + ks * 32 + (lane >> 4) * 8);
    }
    uint2 bv[8];
#pragma unroll
    for (int ct = 0; ct < 8; ++ct) {
      bv[ct] = *reinterpret_cast<const uint2*>(b1 + hh * 128 + ct * 16 + (lane >> 4) * 4);
      if (!half) bv[ct] = make_uint2(0u, 0u);
    }
    __syncthreads();
    if (active) {
      unsigned char* ob = smem + K1_O + wave * (16 * O_STRIDE);
      const unsigned lbase = (unsigned)(size_t)smem + K1_W + ((lane >> 4) * 8 + ((lane & 15) >> 2)) * W_STRIDE + (lane & 3) * 8;
      uint2 o1[8];
#pragma unroll
      for (int ct = 0; ct < 8; ++ct) {
        f32x4 acc0 = {__uint_as_float(bv[ct].x << 16), __uint_as_float(bv[ct].x & 0xffff0000u),
                      __uint_as_float(bv[ct].y << 16), __uint_as_float(bv[ct].y & 0xffff0000u)};
        f32x4 acc1 = acc0;
#pragma unroll
        for (int ks = 0; ks < 4; ++ks) {
          const unsigned a0 = lbase + (ks * 32) * W_STRIDE + ct * 32;
          const s16x4 lo = __builtin_amdgcn_ds_read_tr16_b64_v4i16((lds_s16x4_ptr)(size_t)a0);
          const s16x4 hi = __builtin_amdgcn_ds_read_tr16_b64_v4i16((lds_s16x4_ptr)(size_t)(a0 + 4 * W_STRIDE));
          const bf16x8 a = {lo[0], lo[1], lo[2], lo[3], hi[0], hi[1], hi[2], hi[3]};
          acc0 = __builtin_amdgcn_mfma_f32_16x16x32_bf16(a, xb0[ks], acc0, 0, 0, 0);
          acc1 = __builtin_amdgcn_mfma_f32_16x16x32_bf16(a, xb1[ks], acc1, 0, 0, 0);
        }
        uint2 o;
        o.x = pack2(acc0[0] * NEG_LOG2E, acc0[1] * NEG_LOG2E);
        o.y = pack2(acc0[2] * NEG_LOG2E, acc0[3] * NEG_LOG2E);
        *reinterpret_cast<uint2*>(ob + (lane & 15) * O_STRIDE + ct * 32 + (lane >> 4) * 8) = o;
        o1[ct].x = pack2(acc1[0] * NEG_LOG2E, acc1[1] * NEG_LOG2E);
        o1[ct].y = pack2(acc1[2] * NEG_LOG2E, acc1[3] * NEG_LOG2E);
      }
      unsigned short* dstp = (half ? Qp : Pp) + (size_t)(nt0 * 16) * HH + hh * 128;
#pragma unroll
      for (int r = 0; r < 4; ++r) {
        const int row = r * 4 + (lane >> 4);
        const uint4 v = *reinterpret_cast<const uint4*>(ob + row * O_STRIDE + (lane & 15) * 16);
        *reinterpret_cast<uint4*>(dstp + (size_t)row * HH + (lane & 15) * 8) = v;
      }
      if (nt0 + 1 < NN / 16) {
        __builtin_amdgcn_wave_barrier();
#pragma unroll
        for (int ct = 0; ct < 8; ++ct)
          *reinterpret_cast<uint2*>(ob + (lane & 15) * O_STRIDE + ct * 32 + (lane >> 4) * 8) = o1[ct];
#pragma unroll
        for (int r = 0; r < 4; ++r) {
          const int row = r * 4 + (lane >> 4);
          const uint4 v = *reinterpret_cast<const uint4*>(ob + row * O_STRIDE + (lane & 15) * 16);
          *reinterpret_cast<uint4*>(dstp + (size_t)(16 + row) * HH + (lane & 15) * 8) = v;
        }
      }
    }
  }
}

constexpr int QS_STRIDE = 1040;
constexpr int LCAP = 6144;
constexpr int K2_QS   = 0;
constexpr int K2_Q    = K2_QS + 48 * QS_STRIDE;
constexpr int K2_CAR  = K2_Q + NPB * HH * 2;
constexpr int K2_KEY  = K2_CAR + 16 * HH * 4;
constexpr int K2_CM   = K2_KEY + G * CAP * 4;
constexpr int K2_L    = K2_CM + NPB * G * 4;
constexpr int K2_DEG  = K2_L + (LCAP + 16) * 4;
constexpr int K2_RP   = K2_DEG + 64 * 4;
constexpr int K2_CN   = K2_RP + 64 * 4;
constexpr int K2_BC   = K2_CN + 16 * 4;
constexpr int K2_FLAG = K2_BC + 64 * 4;
constexpr int K2_LDS  = K2_FLAG + 16;

__device__ __forceinline__ float sig_t(float t) {
  return __builtin_amdgcn_rcpf(1.0f + __builtin_amdgcn_exp2f(t));
}

struct Acc { float a0, a1, a2, a3; };

__device__ __forceinline__ void edge_acc(Acc& a, unsigned rec, uint2 v, const float4& q,
                                         float wq0, float wq1, float wq2, float wq3) {
  const float e = __uint_as_float(rec & 0xffff0000u);
  const float t0 = fmaf(e, wq0, q.x) + __uint_as_float(v.x << 16);
  const float t1 = fmaf(e, wq1, q.y) + __uint_as_float(v.x & 0xffff0000u);
  const float t2 = fmaf(e, wq2, q.z) + __uint_as_float(v.y << 16);
  const float t3 = fmaf(e, wq3, q.w) + __uint_as_float(v.y & 0xffff0000u);
  a.a0 = fmaf(t0, sig_t(t0), a.a0);
  a.a1 = fmaf(t1, sig_t(t1), a.a1);
  a.a2 = fmaf(t2, sig_t(t2), a.a2);
  a.a3 = fmaf(t3, sig_t(t3), a.a3);
}

__device__ __forceinline__ float4 load_q(const unsigned char* qrow, int lane) {
  const uint2 v = *reinterpret_cast<const uint2*>(qrow + lane * 8);
  return make_float4(__uint_as_float(v.x << 16), __uint_as_float(v.x & 0xffff0000u),
                     __uint_as_float(v.y << 16), __uint_as_float(v.y & 0xffff0000u));
}

__device__ __forceinline__ int wave_incl_scan(int v, int lane) {
#pragma unroll
  for (int o = 1; o < 64; o <<= 1) {
    const int t = __shfl_up(v, o, 64);
    if (lane >= o) v += t;
  }
  return v;
}

struct Grp { unsigned r0, r1, r2, r3; uint2 v0, v1, v2, v3; };

__device__ __forceinline__ uint2 gather_row(__amdgpu_buffer_rsrc_t rsrc, unsigned loff, unsigned rec) {
  typedef unsigned u32x2v __attribute__((ext_vector_type(2)));
  const u32x2v v = __builtin_amdgcn_raw_buffer_load_b64(rsrc, (int)loff, (int)((rec & 0xffffu) * (HH * 2)), 0);
  return make_uint2(v[0], v[1]);
}

__global__ __launch_bounds__(1024) void k2_agg(
    const unsigned short* __restrict__ Pp, const unsigned short* __restrict__ Qp,
    const uint2* __restrict__ bk, const unsigned* __restrict__ cntg,
    const unsigned short* __restrict__ w2T, const unsigned short* __restrict__ w1,
    const unsigned short* __restrict__ b2,
    const unsigned short* __restrict__ eattr, const int* __restrict__ src, const int* __restrict__ dst,
    unsigned short* __restrict__ out) {
  __shared__ __attribute__((aligned(16))) unsigned char smem[K2_LDS];
  unsigned char* QS = smem + K2_QS;
  unsigned char* Qs = smem + K2_Q;
  float* car = reinterpret_cast<float*>(smem + K2_CAR);
  unsigned* keys = reinterpret_cast<unsigned*>(smem + K2_KEY);
  unsigned* cm = reinterpret_cast<unsigned*>(smem + K2_CM);
  unsigned* L = reinterpret_cast<unsigned*>(smem + K2_L);
  int* degs = reinterpret_cast<int*>(smem + K2_DEG);
  int* rps = reinterpret_cast<int*>(smem + K2_RP);
  int* cnode = reinterpret_cast<int*>(smem + K2_CN);
  unsigned* bcnt = reinterpret_cast<unsigned*>(smem + K2_BC);
  int* flag = reinterpret_cast<int*>(smem + K2_FLAG);

  const int tid = threadIdx.x;
  const int lane = tid & 63;
  const int wave = __builtin_amdgcn_readfirstlane(tid >> 6);
  const int n0 = blockIdx.x * NPB;

  const int mych = tid >> 4;
  const int mysl = (tid & 15) * 4;
  const unsigned mycnt = cntg[blockIdx.x * G + mych];
  uint4 ra, rb;
  {
    const uint4* bp = reinterpret_cast<const uint4*>(bk + ((size_t)blockIdx.x * G + mych) * CAP + mysl);
    ra = bp[0]; rb = bp[1];
  }
  {
    const uint4* qg = reinterpret_cast<const uint4*>(Qp + (size_t)n0 * HH);
    for (int i = tid; i < NPB * HH / 8; i += 1024) reinterpret_cast<uint4*>(Qs)[i] = qg[i];
  }
  float wq0, wq1, wq2, wq3;
  {
    const uint2 wv = *reinterpret_cast<const uint2*>(w1 + (size_t)(2 * DD) * HH + lane * 4);
    wq0 = __uint_as_float(wv.x << 16) * NEG_LOG2E;
    wq1 = __uint_as_float(wv.x & 0xffff0000u) * NEG_LOG2E;
    wq2 = __uint_as_float(wv.y << 16) * NEG_LOG2E;
    wq3 = __uint_as_float(wv.y & 0xffff0000u) * NEG_LOG2E;
  }
  for (int i = tid; i < NPB * G; i += 1024) cm[i] = 0u;
  if (tid == 0) *flag = 0;
  if (tid < 16) cnode[tid] = -1;
  __syncthreads();

  const unsigned rk[4] = {ra.x, ra.z, rb.x, rb.z};
  const unsigned rv[4] = {ra.y, ra.w, rb.y, rb.w};
  if (mycnt > (unsigned)CAP) *flag = 1;
  if ((tid & 15) == 0) bcnt[mych] = mycnt;
  *reinterpret_cast<uint4*>(keys + mych * CAP + mysl) =
      make_uint4((unsigned)(mysl + 0) < mycnt ? rk[0] : 0xffffffffu, (unsigned)(mysl + 1) < mycnt ? rk[1] : 0xffffffffu,
                 (unsigned)(mysl + 2) < mycnt ? rk[2] : 0xffffffffu, (unsigned)(mysl + 3) < mycnt ? rk[3] : 0xffffffffu);
#pragma unroll
  for (int j = 0; j < 4; ++j)
    if ((unsigned)(mysl + j) < mycnt && mycnt <= (unsigned)CAP) atomicAdd(&cm[(rk[j] >> 16) * G + mych], 1u);
  __syncthreads();

#pragma unroll
  for (int s = 0; s < 3; ++s) {
    const int nl = wave + 16 * s;
    if (nl < NPB) {
      const int c = (int)cm[nl * G + lane];
      const int incl = wave_incl_scan(c, lane);
      cm[nl * G + lane] = (unsigned)(incl - c);
      degs[nl] = __builtin_amdgcn_readlane(incl, 63);
    }
  }
  __syncthreads();

  const int mydeg = lane < NPB ? degs[lane] : 0;
  const int mypad = (mydeg + 3) & ~3;
  const int pincl = wave_incl_scan(mypad, lane);
  const int rp = pincl - mypad;
  const int T = __builtin_amdgcn_readlane(pincl, 63);
  const bool fast = (T <= LCAP) && (__builtin_amdgcn_readfirstlane(*flag) == 0);

  if (fast) {
    if (wave == 1) rps[lane] = rp;
    __syncthreads();
    if ((unsigned)mysl < mycnt) {
      unsigned rank[4] = {0u, 0u, 0u, 0u};
      for (unsigned s4 = 0; s4 < mycnt; s4 += 4) {
        const uint4 kq = *reinterpret_cast<const uint4*>(keys + mych * CAP + s4);
#pragma unroll
        for (int j = 0; j < 4; ++j) {
          const unsigned kb = rk[j] & 0xffff0000u, kl = rk[j] & 0xffffu;
          rank[j] += (unsigned)((kq.x - kb) < kl) + (unsigned)((kq.y - kb) < kl) +
                     (unsigned)((kq.z - kb) < kl) + (unsigned)((kq.w - kb) < kl);
        }
      }
#pragma unroll
      for (int j = 0; j < 4; ++j) {
        if ((unsigned)(mysl + j) < mycnt) {
          const unsigned nib = rk[j] >> 16;
          L[rps[nib] + cm[nib * G + mych] + rank[j]] = rv[j];
        }
      }
    }
#pragma unroll
    for (int s = 0; s < 3; ++s) {
      const int nl = wave + 16 * s;
      if (nl < NPB) {
        const int base = __builtin_amdgcn_readlane(rp, nl);
        const int dg = __builtin_amdgcn_readlane(mydeg, nl);
        if (lane < ((dg + 3) & ~3) - dg) L[base + dg + lane] = NULL_REC;
        if (dg == 0) *reinterpret_cast<float4*>(QS + nl * QS_STRIDE + lane * 16) = make_float4(0.f, 0.f, 0.f, 0.f);
      }
    }
    if (wave == 0 && lane < 16) L[T + lane] = NULL_REC;
    __syncthreads();

    const int TG = T >> 2;
    const int G0 = (wave * TG) >> 4;
    const int G1 = ((wave + 1) * TG) >> 4;
    if (G0 < G1) {
      const int sg = rp >> 2;
      int ni = __builtin_popcountll(__ballot(lane < NPB && sg <= G0)) - 1;
      bool mid = __builtin_amdgcn_readlane(sg, ni) < G0;
      int endg = (ni + 1 < NPB ? __builtin_amdgcn_readlane(rp, ni + 1) : T) >> 2;
      float4 q = load_q(Qs + ni * (HH * 2), lane);
      Acc acc = {0.f, 0.f, 0.f, 0.f};
      const unsigned loff = (unsigned)lane * 8u;
      const __amdgpu_buffer_rsrc_t prsrc = __builtin_amdgcn_make_buffer_rsrc(
          const_cast<unsigned short*>(Pp), (short)0, (int)((NN + 1) * HH * 2), 0x00020000);
      unsigned rcur = L[G0 * 4 + lane];
      unsigned rnxt = L[min(G0 * 4 + 64 + lane, LCAP + 15)];

      auto flush = [&]() {
        float* dstrow = mid ? (car + wave * HH) : reinterpret_cast<float*>(QS + ni * QS_STRIDE);
        *reinterpret_cast<float4*>(dstrow + lane * 4) = make_float4(acc.a0, acc.a1, acc.a2, acc.a3);
        if (mid) cnode[wave] = ni;
        mid = false;
        acc.a0 = 0.f; acc.a1 = 0.f; acc.a2 = 0.f; acc.a3 = 0.f;
      };
      auto issue = [&](Grp& b, int g) {
        const int rel = (g - G0) * 4;
        if ((rel & 63) == 0 && rel > 0) {
          rcur = rnxt;
          rnxt = L[min(G0 * 4 + rel + 64 + lane, LCAP + 15)];
        }
        b.r0 = __builtin_amdgcn_readlane(rcur, (rel & 63) + 0);
        b.r1 = __builtin_amdgcn_readlane(rcur, (rel & 63) + 1);
        b.r2 = __builtin_amdgcn_readlane(rcur, (rel & 63) + 2);
        b.r3 = __builtin_amdgcn_readlane(rcur, (rel & 63) + 3);
        b.v0 = gather_row(prsrc, loff, b.r0);
        b.v1 = gather_row(prsrc, loff, b.r1);
        b.v2 = gather_row(prsrc, loff, b.r2);
        b.v3 = gather_row(prsrc, loff, b.r3);
      };
      auto compute = [&](const Grp& b, int g) {
        if (g == endg) {
          flush();
          do {
            ++ni;
            endg = __builtin_amdgcn_readfirstlane((ni + 1 < NPB ? rps[ni + 1] : T) >> 2);
          } while (g == endg);
          q = load_q(Qs + ni * (HH * 2), lane);
        }
        edge_acc(acc, b.r0, b.v0, q, wq0, wq1, wq2, wq3);
        edge_acc(acc, b.r1, b.v1, q, wq0, wq1, wq2, wq3);
        edge_acc(acc, b.r2, b.v2, q, wq0, wq1, wq2, wq3);
        edge_acc(acc, b.r3, b.v3, q, wq0, wq1, wq2, wq3);
      };

      Grp A, B, C;
      issue(A, G0);
      __builtin_amdgcn_sched_barrier(0);
      issue(B, G0 + 1);
      __builtin_amdgcn_sched_barrier(0);
      for (int g = G0; g < G1; g += 3) {
        issue(C, g + 2); compute(A, g);
        issue(A, g + 3); if (g + 1 < G1) compute(B, g + 1);
        issue(B, g + 4); if (g + 2 < G1) compute(C, g + 2);
      }
      flush();
    }
  } else {
#pragma unroll 1
    for (int s = 0; s < 3; ++s) {
      const int nl = wave + 16 * s;
      if (nl < NPB) {
        const float4 q = load_q(Qs + nl * (HH * 2), lane);
        Acc acc = {0.f, 0.f, 0.f, 0.f};
        int total = 0;
        for (int e0 = 0; e0 < EE; e0 += 64) {
          const bool hit = dst[e0 + lane] == n0 + nl;
          unsigned long long m = __ballot(hit);
          if (m != 0ull) {
            const unsigned rec = hit ? (((unsigned)src[e0 + lane] & 0xffffu) | ((unsigned)eattr[e0 + lane] << 16)) : NULL_REC;
            total += __builtin_popcountll(m);
            while (m != 0ull) {
              const int i = __builtin_ctzll(m);
              m &= m - 1ull;
              const unsigned rr = __builtin_amdgcn_readlane(rec, i);
              const uint2 v = *reinterpret_cast<const uint2*>(Pp + (size_t)(rr & 0xffffu) * HH + lane * 4);
              edge_acc(acc, rr, v, q, wq0, wq1, wq2, wq3);
            }
          }
        }
        *reinterpret_cast<float4*>(QS + nl * QS_STRIDE + lane * 16) = make_float4(acc.a0, acc.a1, acc.a2, acc.a3);
        degs[nl] = total;
      }
    }
  }

  const int oct = wave & 7;
  bf16x8 wf[8];
#pragma unroll
  for (int ks = 0; ks < 8; ++ks)
    wf[ks] = *reinterpret_cast<const bf16x8*>(w2T + (oct * 16 + (lane & 15)) * HH + ks * 32 + (lane >> 4) * 8);
  __syncthreads();

#pragma unroll
  for (int s = 0; s < 3; ++s) {
    const int nl = wave + 16 * s;
    if (nl < NPB) {
      unsigned char* row = QS + nl * QS_STRIDE;
      float4 v = *reinterpret_cast<const float4*>(row + lane * 16);
      unsigned long long cm_ = __ballot(lane < 16 && cnode[lane & 15] == nl);
      while (cm_ != 0ull) {
        const int w = __builtin_ctzll(cm_);
        cm_ &= cm_ - 1ull;
        const float4 c = *reinterpret_cast<const float4*>(car + w * HH + lane * 4);
        v.x += c.x; v.y += c.y; v.z += c.z; v.w += c.w;
      }
      const float s0 = v.x * NEG_LN2, s1 = v.y * NEG_LN2, s2 = v.z * NEG_LN2, s3 = v.w * NEG_LN2;
      const unsigned short h0 = f2bf(s0), h1 = f2bf(s1), h2 = f2bf(s2), h3 = f2bf(s3);
      uint2 hv, lv;
      hv.x = (unsigned)h0 | ((unsigned)h1 << 16);
      hv.y = (unsigned)h2 | ((unsigned)h3 << 16);
      lv.x = pack2(s0 - bf2f(h0), s1 - bf2f(h1));
      lv.y = pack2(s2 - bf2f(h2), s3 - bf2f(h3));
      __builtin_amdgcn_wave_barrier();
      *reinterpret_cast<uint2*>(row + lane * 8) = hv;
      *reinterpret_cast<uint2*>(row + 512 + lane * 8) = lv;
    }
  }
  __syncthreads();

  for (int ntile = wave >> 3; ntile < 3; ntile += 2) {
    f32x4 acc = {0.f, 0.f, 0.f, 0.f};
    const int srow = ntile * 16 + (lane & 15);
#pragma unroll
    for (int ks = 0; ks < 8; ++ks) {
      const bf16x8 bh = *reinterpret_cast<const bf16x8*>(QS + srow * QS_STRIDE + ks * 64 + (lane >> 4) * 16);
      const bf16x8 bl = *reinterpret_cast<const bf16x8*>(QS + srow * QS_STRIDE + 512 + ks * 64 + (lane >> 4) * 16);
      acc = __builtin_amdgcn_mfma_f32_16x16x32_bf16(wf[ks], bh, acc, 0, 0, 0);
      acc = __builtin_amdgcn_mfma_f32_16x16x32_bf16(wf[ks], bl, acc, 0, 0, 0);
    }
    if (srow < NPB) {
      const int oc = oct * 16 + (lane >> 4) * 4;
      const float dg = (float)degs[srow];
      uint2 o;
      o.x = pack2(acc[0] + dg * bf2f(b2[oc + 0]), acc[1] + dg * bf2f(b2[oc + 1]));
      o.y = pack2(acc[2] + dg * bf2f(b2[oc + 2]), acc[3] + dg * bf2f(b2[oc + 3]));
      *reinterpret_cast<uint2*>(out + (size_t)(n0 + srow) * DD + oc) = o;
    }
  }
}

}

extern "C" void kernel_launch(void* const* d_in, const int* in_sizes, int n_in,
                              void* d_out, int out_size, void* d_ws, size_t ws_size,
                              hipStream_t stream) {
  const unsigned short* x  = (const unsigned short*)d_in[0];
  const unsigned short* ea = (const unsigned short*)d_in[1];
  const int* src = (const int*)d_in[2];
  const int* dst = (const int*)d_in[3];
  const unsigned short* w1 = (const unsigned short*)d_in[4];
  const unsigned short* b1 = (const unsigned short*)d_in[5];
  const unsigned short* w2 = (const unsigned short*)d_in[6];
  const unsigned short* b2 = (const unsigned short*)d_in[7];
  unsigned char* ws = (unsigned char*)d_ws;
  unsigned short* Pp = (unsigned short*)(ws + WS_P);
  unsigned short* Qp = (unsigned short*)(ws + WS_Q);
  uint2* bk = (uint2*)(ws + WS_BK);
  unsigned* cntg = (unsigned*)(ws + WS_CNT);
  unsigned short* w2T = (unsigned short*)(ws + WS_W2T);

  k1_prep<<<K1_BLOCKS, 1024, 0, stream>>>(x, ea, src, dst, w1, b1, w2, Pp, Qp, bk, cntg, w2T);
  k2_agg<<<NB, 1024, 0, stream>>>(Pp, Qp, bk, cntg, w2T, w1, b2, ea, src, dst, (unsigned short*)d_out);
}
```

```cpp
#include <hip/hip_runtime.h>
#include <hip/hip_bf16.h>
#include <stdint.h>

using bf16x8 = __attribute__((ext_vector_type(8))) short;
using s16x4  = __attribute__((ext_vector_type(4))) short;
using f32x4  = __attribute__((ext_vector_type(4))) float;
typedef __attribute__((address_space(3))) s16x4* lds_s16x4_ptr;

namespace {

constexpr int NN  = 10000;
constexpr int EE  = 320000;
constexpr int DD  = 128;
constexpr int HH  = 256;
constexpr int G   = 64;
constexpr int CH  = EE / G;
constexpr int NPB = 40;
constexpr int NB  = NN / NPB;
constexpr int CAP = 64;
constexpr int GEMM_BLOCKS = 160;
constexpr int K1_BLOCKS = G + GEMM_BLOCKS;
constexpr int W_STRIDE = 288;
constexpr int O_STRIDE = 272;

constexpr size_t WS_P    = 0;
constexpr size_t WS_Q    = WS_P + (size_t)(NN + 1) * HH * 2;
constexpr size_t WS_BK   = WS_Q + (size_t)NN * HH * 2;
constexpr size_t WS_CNT  = WS_BK + (size_t)NB * G * CAP * 8;
constexpr size_t WS_W2T  = WS_CNT + (size_t)NB * G * 4;
constexpr size_t WS_END  = WS_W2T + (size_t)DD * HH * 2;

constexpr float NEG_LOG2E = -1.4426950408889634f;
constexpr float NEG_LN2   = -0.6931471805599453f;
constexpr unsigned NULL_REC = (unsigned)NN;
constexpr unsigned short NULL_VAL = 0x7149;

__device__ __forceinline__ float bf2f(unsigned short u) { return __uint_as_float(((unsigned)u) << 16); }
__device__ __forceinline__ unsigned short f2bf(float f) {
  __hip_bfloat16 h = __float2bfloat16(f);
  return *reinterpret_cast<unsigned short*>(&h);
}
__device__ __forceinline__ unsigned pack2(float a, float b) {
  return (unsigned)f2bf(a) | ((unsigned)f2bf(b) << 16);
}

constexpr int K1_W = 0;
constexpr int K1_O = DD * W_STRIDE;
constexpr int K1_LDS = K1_O + 8 * 16 * O_STRIDE;

__global__ __launch_bounds__(1024) void k1_prep(
    const unsigned short* __restrict__ x, const unsigned short* __restrict__ eattr,
    const int* __restrict__ src, const int* __restrict__ dst,
    const unsigned short* __restrict__ w1, const unsigned short* __restrict__ b1,
    const unsigned short* __restrict__ w2,
    unsigned short* __restrict__ Pp, unsigned short* __restrict__ Qp,
    uint2* __restrict__ bk, unsigned* __restrict__ cntg,
    unsigned short* __restrict__ w2T) {
  __shared__ __attribute__((aligned(16))) unsigned char smem[K1_LDS];
  const int tid = threadIdx.x;
  const int lane = tid & 63;
  const int wave = tid >> 6;
  const int bid = blockIdx.x;

  if (bid < G) {
    unsigned* hist = reinterpret_cast<unsigned*>(smem);
    const int ebase = bid * CH;
    int d[5];
    unsigned rec[5];
#pragma unroll
    for (int i = 0; i < 5; ++i) {
      const int idx = i * 1024 + tid;
      d[i] = 0; rec[i] = 0;
      if (idx < CH) {
        d[i] = dst[ebase + idx];
        rec[i] = ((unsigned)src[ebase + idx] & 0xffffu) | ((unsigned)eattr[ebase + idx] << 16);
      }
    }
    if (tid < 256) hist[tid] = 0u;
    __syncthreads();
#pragma unroll
    for (int i = 0; i < 5; ++i) {
      const int idx = i * 1024 + tid;
      if (idx < CH) {
        const int bin = d[i] / NPB;
        const int nib = d[i] - bin * NPB;
        const unsigned slot = atomicAdd(&hist[bin], 1u);
        if (slot < CAP) bk[((size_t)bin * G + bid) * CAP + slot] = make_uint2(((unsigned)nib << 16) | (unsigned)idx, rec[i]);
      }
    }
    __syncthreads();
    if (tid < NB) cntg[tid * G + bid] = hist[tid];
  } else {
    const int gb = bid - G;
    const int half = gb / 80;
    const int hh = (gb / 40) & 1;
    const int tg = gb % 40;
#pragma unroll
    for (int i = 0; i < 2; ++i) {
      const int k = (tid >> 4) + 64 * i;
      const int c8 = (tid & 15) * 8;
      const uint4 v = *reinterpret_cast<const uint4*>(w1 + (size_t)(half * DD + k) * HH + hh * 128 + c8);
      *reinterpret_cast<uint4*>(smem + K1_W + k * W_STRIDE + c8 * 2) = v;
    }
    if (gb < 64 && tid < 512) {
      const int i = gb * 512 + tid;
      const int k = i >> 7, oc = i & 127;
      w2T[oc * HH + k] = w2[i];
    }
    if (gb == 64 && tid < HH) Pp[(size_t)NN * HH + tid] = NULL_VAL;
    const int nt0 = tg * 16 + wave * 2;
    const bool active = wave < 8 && nt0 < NN / 16;
    const int ntc0 = nt0 < NN / 16 ? nt0 : NN / 16 - 1;
    const int ntc1 = nt0 + 1 < NN / 16 ? nt0 + 1 : NN / 16 - 1;
    bf16x8 xb0[4], xb1[4];
#pragma unroll
    for (int ks = 0; ks < 4; ++ks) {
      xb0[ks] = *reinterpret_cast<const bf16x8*>(x + (size_t)(ntc0 * 16 + (lane & 15)) * DD + ks * 32 + (lane >> 4) * 8);
      xb1[ks] = *reinterpret_cast<const bf16x8*>(x + (size_t)(ntc1 * 16 + (lane & 15)) * DD + ks * 32 + (lane >> 4) * 8);
    }
    uint2 bv[8];
#pragma unroll
    for (int ct = 0; ct < 8; ++ct) {
      bv[ct] = *reinterpret_cast<const uint2*>(b1 + hh * 128 + ct * 16 + (lane >> 4) * 4);
      if (!half) bv[ct] = make_uint2(0u, 0u);
    }
    __syncthreads();
    if (active) {
      unsigned char* ob = smem + K1_O + wave * (16 * O_STRIDE);
      const unsigned lbase = (unsigned)(size_t)smem + K1_W + ((lane >> 4) * 8 + ((lane & 15) >> 2)) * W_STRIDE + (lane & 3) * 8;
      uint2 o1[8];
#pragma unroll
      for (int ct = 0; ct < 8; ++ct) {
        f32x4 acc0 = {__uint_as_float(bv[ct].x << 16), __uint_as_float(bv[ct].x & 0xffff0000u),
                      __uint_as_float(bv[ct].y << 16), __uint_as_float(bv[ct].y & 0xffff0000u)};
        f32x4 acc1 = acc0;
#pragma unroll
        for (int ks = 0; ks < 4; ++ks) {
          const unsigned a0 = lbase + (ks * 32) * W_STRIDE + ct * 32;
          const s16x4 lo = __builtin_amdgcn_ds_read_tr16_b64_v4i16((lds_s16x4_ptr)(size_t)a0);
          const s16x4 hi = __builtin_amdgcn_ds_read_tr16_b64_v4i16((lds_s16x4_ptr)(size_t)(a0 + 4 * W_STRIDE));
          const bf16x8 a = {lo[0], lo[1], lo[2], lo[3], hi[0], hi[1], hi[2], hi[3]};
          acc0 = __builtin_amdgcn_mfma_f32_16x16x32_bf16(a, xb0[ks], acc0, 0, 0, 0);
          acc1 = __builtin_amdgcn_mfma_f32_16x16x32_bf16(a, xb1[ks], acc1, 0, 0, 0);
        }
        uint2 o;
        o.x = pack2(acc0[0] * NEG_LOG2E, acc0[1] * NEG_LOG2E);
        o.y = pack2(acc0[2] * NEG_LOG2E, acc0[3] * NEG_LOG2E);
        *reinterpret_cast<uint2*>(ob + (lane & 15) * O_STRIDE + ct * 32 + (lane >> 4) * 8) = o;
        o1[ct].x = pack2(acc1[0] * NEG_LOG2E, acc1[1] * NEG_LOG2E);
        o1[ct].y = pack2(acc1[2] * NEG_LOG2E, acc1[3] * NEG_LOG2E);
      }
      unsigned short* dstp = (half ? Qp : Pp) + (size_t)(nt0 * 16) * HH + hh * 128;
#pragma unroll
      for (int r = 0; r < 4; ++r) {
        const int row = r * 4 + (lane >> 4);
        const uint4 v = *reinterpret_cast<const uint4*>(ob + row * O_STRIDE + (lane & 15) * 16);
        *reinterpret_cast<uint4*>(dstp + (size_t)row * HH + (lane & 15) * 8) = v;
      }
      if (nt0 + 1 < NN / 16) {
        __builtin_amdgcn_wave_barrier();
#pragma unroll
        for (int ct = 0; ct < 8; ++ct)
          *reinterpret_cast<uint2*>(ob + (lane & 15) * O_STRIDE + ct * 32 + (lane >> 4) * 8) = o1[ct];
#pragma unroll
        for (int r = 0; r < 4; ++r) {
          const int row = r * 4 + (lane >> 4);
          const uint4 v = *reinterpret_cast<const uint4*>(ob + row * O_STRIDE + (lane & 15) * 16);
          *reinterpret_cast<uint4*>(dstp + (size_t)(16 + row) * HH + (lane & 15) * 8) = v;
        }
      }
    }
  }
}

constexpr int QS_STRIDE = 1040;
constexpr int LCAP = 6144;
constexpr int K2_QS   = 0;
constexpr int K2_Q    = K2_QS + 48 * QS_STRIDE;
constexpr int K2_CAR  = K2_Q + NPB * HH * 2;
constexpr int K2_KEY  = K2_CAR + 16 * HH * 4;
constexpr int K2_CM   = K2_KEY + G * CAP * 4;
constexpr int K2_L    = K2_CM + NPB * G * 4;
constexpr int K2_DEG  = K2_L + (LCAP + 16) * 4;
constexpr int K2_RP   = K2_DEG + 64 * 4;
constexpr int K2_CN   = K2_RP + 64 * 4;
constexpr int K2_BC   = K2_CN + 16 * 4;
constexpr int K2_FLAG = K2_BC + 64 * 4;
constexpr int K2_LDS  = K2_FLAG + 16;

__device__ __forceinline__ float sig_t(float t) {
  return __builtin_amdgcn_rcpf(1.0f + __builtin_amdgcn_exp2f(t));
}

struct Acc { float a0, a1, a2, a3; };

__device__ __forceinline__ void edge_acc(Acc& a, unsigned rec, uint2 v, const float4& q,
                                         float wq0, float wq1, float wq2, float wq3) {
  const float e = __uint_as_float(rec & 0xffff0000u);
  const float t0 = fmaf(e, wq0, q.x) + __uint_as_float(v.x << 16);
  const float t1 = fmaf(e, wq1, q.y) + __uint_as_float(v.x & 0xffff0000u);
  const float t2 = fmaf(e, wq2, q.z) + __uint_as_float(v.y << 16);
  const float t3 = fmaf(e, wq3, q.w) + __uint_as_float(v.y & 0xffff0000u);
  a.a0 = fmaf(t0, sig_t(t0), a.a0);
  a.a1 = fmaf(t1, sig_t(t1), a.a1);
  a.a2 = fmaf(t2, sig_t(t2), a.a2);
  a.a3 = fmaf(t3, sig_t(t3), a.a3);
}

__device__ __forceinline__ float4 load_q(const unsigned char* qrow, int lane) {
  const uint2 v = *reinterpret_cast<const uint2*>(qrow + lane * 8);
  return make_float4(__uint_as_float(v.x << 16), __uint_as_float(v.x & 0xffff0000u),
                     __uint_as_float(v.y << 16), __uint_as_float(v.y & 0xffff0000u));
}

__device__ __forceinline__ int wave_incl_scan(int v, int lane) {
#pragma unroll
  for (int o = 1; o < 64; o <<= 1) {
    const int t = __shfl_up(v, o, 64);
    if (lane >= o) v += t;
  }
  return v;
}

struct Grp { unsigned r0, r1, r2, r3; uint2 v0, v1, v2, v3; };

__device__ __forceinline__ uint2 gather_row(__amdgpu_buffer_rsrc_t rsrc, unsigned loff, unsigned rec) {
  typedef unsigned u32x2v __attribute__((ext_vector_type(2)));
  const u32x2v v = __builtin_amdgcn_raw_buffer_load_b64(rsrc, (int)loff, (int)((rec & 0xffffu) * (HH * 2)), 0);
  return make_uint2(v[0], v[1]);
}

__global__ __launch_bounds__(1024) void k2_agg(
    const unsigned short* __restrict__ Pp, const unsigned short* __restrict__ Qp,
    const uint2* __restrict__ bk, const unsigned* __restrict__ cntg,
    const unsigned short* __restrict__ w2T, const unsigned short* __restrict__ w1,
    const unsigned short* __restrict__ b2,
    const unsigned short* __restrict__ eattr, const int* __restrict__ src, const int* __restrict__ dst,
    unsigned short* __restrict__ out) {
  __shared__ __attribute__((aligned(16))) unsigned char smem[K2_LDS];
  unsigned char* QS = smem + K2_QS;
  unsigned char* Qs = smem + K2_Q;
  float* car = reinterpret_cast<float*>(smem + K2_CAR);
  unsigned* keys = reinterpret_cast<unsigned*>(smem + K2_KEY);
  unsigned* cm = reinterpret_cast<unsigned*>(smem + K2_CM);
  unsigned* L = reinterpret_cast<unsigned*>(smem + K2_L);
  int* degs = reinterpret_cast<int*>(smem + K2_DEG);
  int* rps = reinterpret_cast<int*>(smem + K2_RP);
  int* cnode = reinterpret_cast<int*>(smem + K2_CN);
  unsigned* bcnt = reinterpret_cast<unsigned*>(smem + K2_BC);
  int* flag = reinterpret_cast<int*>(smem + K2_FLAG);

  const int tid = threadIdx.x;
  const int lane = tid & 63;
  const int wave = __builtin_amdgcn_readfirstlane(tid >> 6);
  const int n0 = blockIdx.x * NPB;

  const int mych = tid >> 4;
  const int mysl = tid & 15;
  const unsigned mycnt = cntg[blockIdx.x * G + mych];
  uint2 rr4[4];
  {
    const uint2* bp = bk + ((size_t)blockIdx.x * G + mych) * CAP + mysl;
#pragma unroll
    for (int j = 0; j < 4; ++j) rr4[j] = bp[16 * j];
  }
  {
    const uint4* qg = reinterpret_cast<const uint4*>(Qp + (size_t)n0 * HH);
    for (int i = tid; i < NPB * HH / 8; i += 1024) reinterpret_cast<uint4*>(Qs)[i] = qg[i];
  }
  float wq0, wq1, wq2, wq3;
  {
    const uint2 wv = *reinterpret_cast<const uint2*>(w1 + (size_t)(2 * DD) * HH + lane * 4);
    wq0 = __uint_as_float(wv.x << 16) * NEG_LOG2E;
    wq1 = __uint_as_float(wv.x & 0xffff0000u) * NEG_LOG2E;
    wq2 = __uint_as_float(wv.y << 16) * NEG_LOG2E;
    wq3 = __uint_as_float(wv.y & 0xffff0000u) * NEG_LOG2E;
  }
  for (int i = tid; i < NPB * G; i += 1024) cm[i] = 0u;
  if (tid == 0) *flag = 0;
  if (tid < 16) cnode[tid] = -1;
  __syncthreads();

  unsigned rk[4], rv[4];
#pragma unroll
  for (int j = 0; j < 4; ++j) {
    const bool ok = (unsigned)(mysl + 16 * j) < mycnt;
    rk[j] = ok ? rr4[j].x : 0xffffffffu;
    rv[j] = rr4[j].y;
  }
  if (mycnt > (unsigned)CAP) *flag = 1;
#pragma unroll
  for (int j = 0; j < 4; ++j) {
    keys[mych * CAP + mysl + 16 * j] = rk[j];
    if (rk[j] != 0xffffffffu && mycnt <= (unsigned)CAP) atomicAdd(&cm[(rk[j] >> 16) * G + mych], 1u);
  }
  __syncthreads();

#pragma unroll
  for (int s = 0; s < 3; ++s) {
    const int nl = wave + 16 * s;
    if (nl < NPB) {
      const int c = (int)cm[nl * G + lane];
      const int incl = wave_incl_scan(c, lane);
      cm[nl * G + lane] = (unsigned)(incl - c);
      degs[nl] = __builtin_amdgcn_readlane(incl, 63);
    }
  }
  __syncthreads();

  const int mydeg = lane < NPB ? degs[lane] : 0;
  const int mypad = (mydeg + 3) & ~3;
  const int pincl = wave_incl_scan(mypad, lane);
  const int rp = pincl - mypad;
  const int T = __builtin_amdgcn_readlane(pincl, 63);
  const bool fast = (T <= LCAP) && (__builtin_amdgcn_readfirstlane(*flag) == 0);

  if (fast) {
    if (wave == 1) rps[lane] = rp;
    {
      unsigned mx = mycnt;
#pragma unroll
      for (int o = 16; o < 64; o <<= 1) mx = max(mx, (unsigned)__shfl_xor((int)mx, o, 64));
      const int nrow = __builtin_amdgcn_readfirstlane((int)((mx + 15u) >> 4));
      unsigned rank[4] = {0u, 0u, 0u, 0u};
      const int nq = __builtin_amdgcn_readfirstlane((int)((mx + 3u) >> 2));
      for (int s4 = 0; s4 < nq; ++s4) {
        const uint4 kq = *reinterpret_cast<const uint4*>(keys + mych * CAP + s4 * 4);
#pragma unroll
        for (int j = 0; j < 4; ++j) {
          if (j < nrow) {
            const unsigned kb = rk[j] & 0xffff0000u, kl = rk[j] & 0xffffu;
            rank[j] += (unsigned)((kq.x - kb) < kl) + (unsigned)((kq.y - kb) < kl) +
                       (unsigned)((kq.z - kb) < kl) + (unsigned)((kq.w - kb) < kl);
          }
        }
      }
#pragma unroll
      for (int j = 0; j < 4; ++j) {
        if (j < nrow) {
          const unsigned nib = min(rk[j] >> 16, (unsigned)(NPB - 1));
          const int base = __shfl(rp, (int)nib, 64);
          if (rk[j] != 0xffffffffu) L[base + cm[nib * G + mych] + rank[j]] = rv[j];
        }
      }
    }
#pragma unroll
    for (int s = 0; s < 3; ++s) {
      const int nl = wave + 16 * s;
      if (nl < NPB) {
        const int base = __builtin_amdgcn_readlane(rp, nl);
        const int dg = __builtin_amdgcn_readlane(mydeg, nl);
        if (lane < ((dg + 3) & ~3) - dg) L[base + dg + lane] = NULL_REC;
        if (dg == 0) *reinterpret_cast<float4*>(QS + nl * QS_STRIDE + lane * 16) = make_float4(0.f, 0.f, 0.f, 0.f);
      }
    }
    if (wave == 0 && lane < 16) L[T + lane] = NULL_REC;
    __syncthreads();

    const int TG = T >> 2;
    const int G0 = (wave * TG) >> 4;
    const int G1 = ((wave + 1) * TG) >> 4;
    if (G0 < G1) {
      const int sg = rp >> 2;
      int ni = __builtin_popcountll(__ballot(lane < NPB && sg <= G0)) - 1;
      bool mid = __builtin_amdgcn_readlane(sg, ni) < G0;
      int endg = (ni + 1 < NPB ? __builtin_amdgcn_readlane(rp, ni + 1) : T) >> 2;
      float4 q = load_q(Qs + ni * (HH * 2), lane);
      Acc acc = {0.f, 0.f, 0.f, 0.f};
      const unsigned loff = (unsigned)lane * 8u;
      const __amdgpu_buffer_rsrc_t prsrc = __builtin_amdgcn_make_buffer_rsrc(
          const_cast<unsigned short*>(Pp), (short)0, (int)((NN + 1) * HH * 2), 0x00020000);
      unsigned rcur = L[G0 * 4 + lane];
      unsigned rnxt = L[min(G0 * 4 + 64 + lane, LCAP + 15)];

      auto flush = [&]() {
        float* dstrow = mid ? (car + wave * HH) : reinterpret_cast<float*>(QS + ni * QS_STRIDE);
        *reinterpret_cast<float4*>(dstrow + lane * 4) = make_float4(acc.a0, acc.a1, acc.a2, acc.a3);
        if (mid) cnode[wave] = ni;
        mid = false;
        acc.a0 = 0.f; acc.a1 = 0.f; acc.a2 = 0.f; acc.a3 = 0.f;
      };
      auto issue = [&](Grp& b, int g) {
        const int rel = (g - G0) * 4;
        if ((rel & 63) == 0 && rel > 0) {
          rcur = rnxt;
          rnxt = L[min(G0 * 4 + rel + 64 + lane, LCAP + 15)];
        }
        b.r0 = __builtin_amdgcn_readlane(rcur, (rel & 63) + 0);
        b.r1 = __builtin_amdgcn_readlane(rcur, (rel & 63) + 1);
        b.r2 = __builtin_amdgcn_readlane(rcur, (rel & 63) + 2);
        b.r3 = __builtin_amdgcn_readlane(rcur, (rel & 63) + 3);
        b.v0 = gather_row(prsrc, loff, b.r0);
        b.v1 = gather_row(prsrc, loff, b.r1);
        b.v2 = gather_row(prsrc, loff, b.r2);
        b.v3 = gather_row(prsrc, loff, b.r3);
      };
      auto compute = [&](const Grp& b, int g) {
        if (g == endg) {
          flush();
          do {
            ++ni;
            endg = __builtin_amdgcn_readfirstlane((ni + 1 < NPB ? rps[ni + 1] : T) >> 2);
          } while (g == endg);
          q = load_q(Qs + ni * (HH * 2), lane);
        }
        edge_acc(acc, b.r0, b.v0, q, wq0, wq1, wq2, wq3);
        edge_acc(acc, b.r1, b.v1, q, wq0, wq1, wq2, wq3);
        edge_acc(acc, b.r2, b.v2, q, wq0, wq1, wq2, wq3);
        edge_acc(acc, b.r3, b.v3, q, wq0, wq1, wq2, wq3);
      };

      Grp A, B, C;
      issue(A, G0);
      __builtin_amdgcn_sched_barrier(0);
      issue(B, G0 + 1);
      __builtin_amdgcn_sched_barrier(0);
      for (int g = G0; g < G1; g += 3) {
        issue(C, g + 2); compute(A, g);
        issue(A, g + 3); if (g + 1 < G1) compute(B, g + 1);
        issue(B, g + 4); if (g + 2 < G1) compute(C, g + 2);
      }
      flush();
    }
  } else {
#pragma unroll 1
    for (int s = 0; s < 3; ++s) {
      const int nl = wave + 16 * s;
      if (nl < NPB) {
        const float4 q = load_q(Qs + nl * (HH * 2), lane);
        Acc acc = {0.f, 0.f, 0.f, 0.f};
        int total = 0;
        for (int e0 = 0; e0 < EE; e0 += 64) {
          const bool hit = dst[e0 + lane] == n0 + nl;
          unsigned long long m = __ballot(hit);
          if (m != 0ull) {
            const unsigned rec = hit ? (((unsigned)src[e0 + lane] & 0xffffu) | ((unsigned)eattr[e0 + lane] << 16)) : NULL_REC;
            total += __builtin_popcountll(m);
            while (m != 0ull) {
              const int i = __builtin_ctzll(m);
              m &= m - 1ull;
              const unsigned rr = __builtin_amdgcn_readlane(rec, i);
              const uint2 v = *reinterpret_cast<const uint2*>(Pp + (size_t)(rr & 0xffffu) * HH + lane * 4);
              edge_acc(acc, rr, v, q, wq0, wq1, wq2, wq3);
            }
          }
        }
        *reinterpret_cast<float4*>(QS + nl * QS_STRIDE + lane * 16) = make_float4(acc.a0, acc.a1, acc.a2, acc.a3);
        degs[nl] = total;
      }
    }
  }

  const int oct = wave & 7;
  bf16x8 wf[8];
#pragma unroll
  for (int ks = 0; ks < 8; ++ks)
    wf[ks] = *reinterpret_cast<const bf16x8*>(w2T + (oct * 16 + (lane & 15)) * HH + ks * 32 + (lane >> 4) * 8);
  __syncthreads();

#pragma unroll
  for (int s = 0; s < 3; ++s) {
    const int nl = wave + 16 * s;
    if (nl < NPB) {
      unsigned char* row = QS + nl * QS_STRIDE;
      float4 v = *reinterpret_cast<const float4*>(row + lane * 16);
      unsigned long long cm_ = __ballot(lane < 16 && cnode[lane & 15] == nl);
      while (cm_ != 0ull) {
        const int w = __builtin_ctzll(cm_);
        cm_ &= cm_ - 1ull;
        const float4 c = *reinterpret_cast<const float4*>(car + w * HH + lane * 4);
        v.x += c.x; v.y += c.y; v.z += c.z; v.w += c.w;
      }
      const float s0 = v.x * NEG_LN2, s1 = v.y * NEG_LN2, s2 = v.z * NEG_LN2, s3 = v.w * NEG_LN2;
      const unsigned short h0 = f2bf(s0), h1 = f2bf(s1), h2 = f2bf(s2), h3 = f2bf(s3);
      uint2 hv, lv;
      hv.x = (unsigned)h0 | ((unsigned)h1 << 16);
      hv.y = (unsigned)h2 | ((unsigned)h3 << 16);
      lv.x = pack2(s0 - bf2f(h0), s1 - bf2f(h1));
      lv.y = pack2(s2 - bf2f(h2), s3 - bf2f(h3));
      __builtin_amdgcn_wave_barrier();
      *reinterpret_cast<uint2*>(row + lane * 8) = hv;
      *reinterpret_cast<uint2*>(row + 512 + lane * 8) = lv;
    }
  }
  __syncthreads();

  for (int ntile = wave >> 3; ntile < 3; ntile += 2) {
    f32x4 acc = {0.f, 0.f, 0.f, 0.f};
    const int srow = ntile * 16 + (lane & 15);
#pragma unroll
    for (int ks = 0; ks < 8; ++ks) {
      const bf16x8 bh = *reinterpret_cast<const bf16x8*>(QS + srow * QS_STRIDE + ks * 64 + (lane >> 4) * 16);
      const bf16x8 bl = *reinterpret_cast<const bf16x8*>(QS + srow * QS_STRIDE + 512 + ks * 64 + (lane >> 4) * 16);
      acc = __builtin_amdgcn_mfma_f32_16x16x32_bf16(wf[ks], bh, acc, 0, 0, 0);
      acc = __builtin_amdgcn_mfma_f32_16x16x32_bf16(wf[ks], bl, acc, 0, 0, 0);
    }
    if (srow < NPB) {
      const int oc = oct * 16 + (lane >> 4) * 4;
      const float dg = (float)degs[srow];
      uint2 o;
      o.x = pack2(acc[0] + dg * bf2f(b2[oc + 0]), acc[1] + dg * bf2f(b2[oc + 1]));
      o.y = pack2(acc[2] + dg * bf2f(b2[oc + 2]), acc[3] + dg * bf2f(b2[oc + 3]));
      *reinterpret_cast<uint2*>(out + (size_t)(n0 + srow) * DD + oc) = o;
    }
  }
}

}

extern "C" void kernel_launch(void* const* d_in, const int* in_sizes, int n_in,
                              void* d_out, int out_size, void* d_ws, size_t ws_size,
                              hipStream_t stream) {
  const unsigned short* x  = (const unsigned short*)d_in[0];
  const unsigned short* ea = (const unsigned short*)d_in[1];
  const int* src = (const int*)d_in[2];
  const int* dst = (const int*)d_in[3];
  const unsigned short* w1 = (const unsigned short*)d_in[4];
  const unsigned short* b1 = (const unsigned short*)d_in[5];
  const unsigned short* w2 = (const unsigned short*)d_in[6];
  const unsigned short* b2 = (const unsigned short*)d_in[7];
  unsigned char* ws = (unsigned char*)d_ws;
  unsigned short* Pp = (unsigned short*)(ws + WS_P);
  unsigned short* Qp = (unsigned short*)(ws + WS_Q);
  uint2* bk = (uint2*)(ws + WS_BK);
  unsigned* cntg = (unsigned*)(ws + WS_CNT);
  unsigned short* w2T = (unsigned short*)(ws + WS_W2T);

  k1_prep<<<K1_BLOCKS, 1024, 0, stream>>>(x, ea, src, dst, w1, b1, w2, Pp, Qp, bk, cntg, w2T);
  k2_agg<<<NB, 1024, 0, stream>>>(Pp, Qp, bk, cntg, w2T, w1, b2, ea, src, dst, (unsigned short*)d_out);
}
```

```cpp
#include <hip/hip_runtime.h>
#include <hip/hip_bf16.h>
#include <stdint.h>

using bf16x8 = __attribute__((ext_vector_type(8))) short;
using s16x4  = __attribute__((ext_vector_type(4))) short;
using f32x4  = __attribute__((ext_vector_type(4))) float;
typedef __attribute__((address_space(3))) s16x4* lds_s16x4_ptr;

namespace {

constexpr int NN  = 10000;
constexpr int EE  = 320000;
constexpr int DD  = 128;
constexpr int HH  = 256;
constexpr int G   = 64;
constexpr int CH  = EE / G;
constexpr int NPB = 40;
constexpr int NB  = NN / NPB;
constexpr int CAP = 64;
constexpr int GEMM_BLOCKS = 160;
constexpr int K1_BLOCKS = G + GEMM_BLOCKS;
constexpr int W_STRIDE = 288;
constexpr int O_STRIDE = 272;

constexpr size_t WS_P    = 0;
constexpr size_t WS_Q    = WS_P + (size_t)(NN + 1) * HH * 2;
constexpr size_t WS_BK   = WS_Q + (size_t)NN * HH * 2;
constexpr size_t WS_CNT  = WS_BK + (size_t)NB * G * CAP * 8;
constexpr size_t WS_W2T  = WS_CNT + (size_t)NB * G * 4;
constexpr size_t WS_END  = WS_W2T + (size_t)DD * HH * 2;

constexpr float NEG_LOG2E = -1.4426950408889634f;
constexpr float NEG_LN2   = -0.6931471805599453f;
constexpr unsigned NULL_REC = (unsigned)NN;
constexpr unsigned short NULL_VAL = 0x7149;

__device__ __forceinline__ float bf2f(unsigned short u) { return __uint_as_float(((unsigned)u) << 16); }
__device__ __forceinline__ unsigned short f2bf(float f) {
  __hip_bfloat16 h = __float2bfloat16(f);
  return *reinterpret_cast<unsigned short*>(&h);
}
__device__ __forceinline__ unsigned pack2(float a, float b) {
  return (unsigned)f2bf(a) | ((unsigned)f2bf(b) << 16);
}

constexpr int K1_W = 0;
constexpr int K1_O = DD * W_STRIDE;
constexpr int K1_LDS = K1_O + 8 * 16 * O_STRIDE;

__global__ __launch_bounds__(1024) void k1_prep(
    const unsigned short* __restrict__ x, const unsigned short* __restrict__ eattr,
    const int* __restrict__ src, const int* __restrict__ dst,
    const unsigned short* __restrict__ w1, const unsigned short* __restrict__ b1,
    const unsigned short* __restrict__ w2,
    unsigned short* __restrict__ Pp, unsigned short* __restrict__ Qp,
    uint2* __restrict__ bk, unsigned* __restrict__ cntg,
    unsigned short* __restrict__ w2T) {
  __shared__ __attribute__((aligned(16))) unsigned char smem[K1_LDS];
  const int tid = threadIdx.x;
  const int lane = tid & 63;
  const int wave = tid >> 6;
  const int bid = blockIdx.x;

  if (bid < G) {
    unsigned* hist = reinterpret_cast<unsigned*>(smem);
    const int ebase = bid * CH;
    int d[5];
    unsigned rec[5];
#pragma unroll
    for (int i = 0; i < 5; ++i) {
      const int idx = i * 1024 + tid;
      d[i] = 0; rec[i] = 0;
      if (idx < CH) {
        d[i] = dst[ebase + idx];
        rec[i] = ((unsigned)src[ebase + idx] & 0xffffu) | ((unsigned)eattr[ebase + idx] << 16);
      }
    }
    if (tid < 256) hist[tid] = 0u;
    __syncthreads();
#pragma unroll
    for (int i = 0; i < 5; ++i) {
      const int idx = i * 1024 + tid;
      if (idx < CH) {
        const int bin = d[i] / NPB;
        const int nib = d[i] - bin * NPB;
        const unsigned slot = atomicAdd(&hist[bin], 1u);
        if (slot < CAP) bk[((size_t)bin * G + bid) * CAP + slot] = make_uint2(((unsigned)nib << 16) | (unsigned)idx, rec[i]);
      }
    }
    __syncthreads();
    if (tid < NB) cntg[tid * G + bid] = hist[tid];
  } else {
    const int gb = bid - G;
    const int half = gb / 80;
    const int hh = (gb / 40) & 1;
    const int tg = gb % 40;
#pragma unroll
    for (int i = 0; i < 2; ++i) {
      const int k = (tid >> 4) + 64 * i;
      const int c8 = (tid & 15) * 8;
      const uint4 v = *reinterpret_cast<const uint4*>(w1 + (size_t)(half * DD + k) * HH + hh * 128 + c8);
      *reinterpret_cast<uint4*>(smem + K1_W + k * W_STRIDE + c8 * 2) = v;
    }
    if (gb < 64 && tid >= 512) {
      const int i = gb * 512 + tid - 512;
      const int k = i >> 7, oc = i & 127;
      w2T[oc * HH + k] = w2[i];
    }
    if (gb == 64 && tid >= 512 && tid < 512 + HH) Pp[(size_t)NN * HH + tid - 512] = NULL_VAL;
    const int nt0 = tg * 16 + wave * 2;
    const bool active = wave < 8 && nt0 < NN / 16;
    const int ntc0 = nt0 < NN / 16 ? nt0 : NN / 16 - 1;
    const int ntc1 = nt0 + 1 < NN / 16 ? nt0 + 1 : NN / 16 - 1;
    bf16x8 xb0[4], xb1[4];
    uint2 bv[8];
    if (active) {
#pragma unroll
      for (int ks = 0; ks < 4; ++ks) {
        xb0[ks] = *reinterpret_cast<const bf16x8*>(x + (size_t)(ntc0 * 16 + (lane & 15)) * DD + ks * 32 + (lane >> 4) * 8);
        xb1[ks] = *reinterpret_cast<const bf16x8*>(x + (size_t)(ntc1 * 16 + (lane & 15)) * DD + ks * 32 + (lane >> 4) * 8);
      }
#pragma unroll
      for (int ct = 0; ct < 8; ++ct) {
        bv[ct] = *reinterpret_cast<const uint2*>(b1 + hh * 128 + ct * 16 + (lane >> 4) * 4);
        if (!half) bv[ct] = make_uint2(0u, 0u);
      }
    }
    __syncthreads();
    if (active) {
      unsigned char* ob = smem + K1_O + wave * (16 * O_STRIDE);
      const unsigned lbase = (unsigned)(size_t)smem + K1_W + ((lane >> 4) * 8 + ((lane & 15) >> 2)) * W_STRIDE + (lane & 3) * 8;
      uint2 o1[8];
#pragma unroll
      for (int ct = 0; ct < 8; ++ct) {
        f32x4 acc0 = {__uint_as_float(bv[ct].x << 16), __uint_as_float(bv[ct].x & 0xffff0000u),
                      __uint_as_float(bv[ct].y << 16), __uint_as_float(bv[ct].y & 0xffff0000u)};
        f32x4 acc1 = acc0;
#pragma unroll
        for (int ks = 0; ks < 4; ++ks) {
          const unsigned a0 = lbase + (ks * 32) * W_STRIDE + ct * 32;
          const s16x4 lo = __builtin_amdgcn_ds_read_tr16_b64_v4i16((lds_s16x4_ptr)(size_t)a0);
          const s16x4 hi = __builtin_amdgcn_ds_read_tr16_b64_v4i16((lds_s16x4_ptr)(size_t)(a0 + 4 * W_STRIDE));
          const bf16x8 a = {lo[0], lo[1], lo[2], lo[3], hi[0], hi[1], hi[2], hi[3]};
          acc0 = __builtin_amdgcn_mfma_f32_16x16x32_bf16(a, xb0[ks], acc0, 0, 0, 0);
          acc1 = __builtin_amdgcn_mfma_f32_16x16x32_bf16(a, xb1[ks], acc1, 0, 0, 0);
        }
        uint2 o;
        o.x = pack2(acc0[0] * NEG_LOG2E, acc0[1] * NEG_LOG2E);
        o.y = pack2(acc0[2] * NEG_LOG2E, acc0[3] * NEG_LOG2E);
        *reinterpret_cast<uint2*>(ob + (lane & 15) * O_STRIDE + ct * 32 + (lane >> 4) * 8) = o;
        o1[ct].x = pack2(acc1[0] * NEG_LOG2E, acc1[1] * NEG_LOG2E);
        o1[ct].y = pack2(acc1[2] * NEG_LOG2E, acc1[3] * NEG_LOG2E);
      }
      unsigned short* dstp = (half ? Qp : Pp) + (size_t)(nt0 * 16) * HH + hh * 128;
#pragma unroll
      for (int r = 0; r < 4; ++r) {
        const int row = r * 4 + (lane >> 4);
        const uint4 v = *reinterpret_cast<const uint4*>(ob + row * O_STRIDE + (lane & 15) * 16);
        *reinterpret_cast<uint4*>(dstp + (size_t)row * HH + (lane & 15) * 8) = v;
      }
      if (nt0 + 1 < NN / 16) {
        __builtin_amdgcn_wave_barrier();
#pragma unroll
        for (int ct = 0; ct < 8; ++ct)
          *reinterpret_cast<uint2*>(ob + (lane & 15) * O_STRIDE + ct * 32 + (lane >> 4) * 8) = o1[ct];
#pragma unroll
        for (int r = 0; r < 4; ++r) {
          const int row = r * 4 + (lane >> 4);
          const uint4 v = *reinterpret_cast<const uint4*>(ob + row * O_STRIDE + (lane & 15) * 16);
          *reinterpret_cast<uint4*>(dstp + (size_t)(16 + row) * HH + (lane & 15) * 8) = v;
        }
      }
    }
  }
}

constexpr int QS_STRIDE = 1040;
constexpr int LCAP = 6144;
constexpr int K2_QS   = 0;
constexpr int K2_Q    = K2_QS + 48 * QS_STRIDE;
constexpr int K2_CAR  = K2_Q + NPB * HH * 2;
constexpr int K2_KEY  = K2_CAR + 16 * HH * 4;
constexpr int K2_CM   = K2_KEY + G * CAP * 4;
constexpr int K2_L    = K2_CM + NPB * G * 4;
constexpr int K2_DEG  = K2_L + (LCAP + 32) * 4;
constexpr int K2_RP   = K2_DEG + 64 * 4;
constexpr int K2_CN   = K2_RP + 64 * 4;
constexpr int K2_BC   = K2_CN + 16 * 4;
constexpr int K2_FLAG = K2_BC + 64 * 4;
constexpr int K2_LDS  = K2_FLAG + 16;

__device__ __forceinline__ float sig_t(float t) {
  return __builtin_amdgcn_rcpf(1.0f + __builtin_amdgcn_exp2f(t));
}

struct Acc { float a0, a1, a2, a3; };

__device__ __forceinline__ void edge_acc(Acc& a, unsigned rec, uint2 v, const float4& q,
                                         float wq0, float wq1, float wq2, float wq3) {
  const float e = __uint_as_float(rec & 0xffff0000u);
  const float t0 = fmaf(e, wq0, q.x) + __uint_as_float(v.x << 16);
  const float t1 = fmaf(e, wq1, q.y) + __uint_as_float(v.x & 0xffff0000u);
  const float t2 = fmaf(e, wq2, q.z) + __uint_as_float(v.y << 16);
  const float t3 = fmaf(e, wq3, q.w) + __uint_as_float(v.y & 0xffff0000u);
  a.a0 = fmaf(t0, sig_t(t0), a.a0);
  a.a1 = fmaf(t1, sig_t(t1), a.a1);
  a.a2 = fmaf(t2, sig_t(t2), a.a2);
  a.a3 = fmaf(t3, sig_t(t3), a.a3);
}

__device__ __forceinline__ float4 load_q(const unsigned char* qrow, int lane) {
  const uint2 v = *reinterpret_cast<const uint2*>(qrow + lane * 8);
  return make_float4(__uint_as_float(v.x << 16), __uint_as_float(v.x & 0xffff0000u),
                     __uint_as_float(v.y << 16), __uint_as_float(v.y & 0xffff0000u));
}

__device__ __forceinline__ int wave_incl_scan(int v, int lane) {
#pragma unroll
  for (int o = 1; o < 64; o <<= 1) {
    const int t = __shfl_up(v, o, 64);
    if (lane >= o) v += t;
  }
  return v;
}

struct Grp { unsigned r0, r1, r2, r3; uint2 v0, v1, v2, v3; };

__device__ __forceinline__ uint2 gather_row(__amdgpu_buffer_rsrc_t rsrc, unsigned loff, unsigned rec) {
  typedef unsigned u32x2v __attribute__((ext_vector_type(2)));
  const u32x2v v = __builtin_amdgcn_raw_buffer_load_b64(rsrc, (int)loff, (int)((rec & 0xffffu) * (HH * 2)), 0);
  return make_uint2(v[0], v[1]);
}

__global__ __launch_bounds__(1024) void k2_agg(
    const unsigned short* __restrict__ Pp, const unsigned short* __restrict__ Qp,
    const uint2* __restrict__ bk, const unsigned* __restrict__ cntg,
    const unsigned short* __restrict__ w2T, const unsigned short* __restrict__ w1,
    const unsigned short* __restrict__ b2,
    const unsigned short* __restrict__ eattr, const int* __restrict__ src, const int* __restrict__ dst,
    unsigned short* __restrict__ out) {
  __shared__ __attribute__((aligned(16))) unsigned char smem[K2_LDS];
  unsigned char* QS = smem + K2_QS;
  unsigned char* Qs = smem + K2_Q;
  float* car = reinterpret_cast<float*>(smem + K2_CAR);
  unsigned* keys = reinterpret_cast<unsigned*>(smem + K2_KEY);
  unsigned* cm = reinterpret_cast<unsigned*>(smem + K2_CM);
  unsigned* L = reinterpret_cast<unsigned*>(smem + K2_L);
  int* degs = reinterpret_cast<int*>(smem + K2_DEG);
  int* rps = reinterpret_cast<int*>(smem + K2_RP);
  int* cnode = reinterpret_cast<int*>(smem + K2_CN);
  unsigned* bcnt = reinterpret_cast<unsigned*>(smem + K2_BC);
  int* flag = reinterpret_cast<int*>(smem + K2_FLAG);

  const int tid = threadIdx.x;
  const int lane = tid & 63;
  const int wave = __builtin_amdgcn_readfirstlane(tid >> 6);
  const int n0 = blockIdx.x * NPB;

  const int mych = tid >> 4;
  const int mysl = tid & 15;
  const unsigned mycnt = cntg[blockIdx.x * G + mych];
  uint2 rr4[4];
  {
    const uint2* bp = bk + ((size_t)blockIdx.x * G + mych) * CAP + mysl;
#pragma unroll
    for (int j = 0; j < 4; ++j) rr4[j] = bp[16 * j];
  }
  {
    const uint4* qg = reinterpret_cast<const uint4*>(Qp + (size_t)n0 * HH);
    for (int i = tid; i < NPB * HH / 8; i += 1024) reinterpret_cast<uint4*>(Qs)[i] = qg[i];
  }
  float wq0, wq1, wq2, wq3;
  {
    const uint2 wv = *reinterpret_cast<const uint2*>(w1 + (size_t)(2 * DD) * HH + lane * 4);
    wq0 = __uint_as_float(wv.x << 16) * NEG_LOG2E;
    wq1 = __uint_as_float(wv.x & 0xffff0000u) * NEG_LOG2E;
    wq2 = __uint_as_float(wv.y << 16) * NEG_LOG2E;
    wq3 = __uint_as_float(wv.y & 0xffff0000u) * NEG_LOG2E;
  }
  for (int i = tid; i < NPB * G; i += 1024) cm[i] = 0u;
  if (tid == 0) *flag = 0;
  if (tid < 16) cnode[tid] = -1;
  __syncthreads();

  unsigned rk[4], rv[4];
#pragma unroll
  for (int j = 0; j < 4; ++j) {
    const bool ok = (unsigned)(mysl + 16 * j) < mycnt;
    rk[j] = ok ? rr4[j].x : 0xffffffffu;
    rv[j] = rr4[j].y;
  }
  if (mycnt > (unsigned)CAP) *flag = 1;
#pragma unroll
  for (int j = 0; j < 4; ++j) {
    keys[mych * CAP + mysl + 16 * j] = rk[j];
    if (rk[j] != 0xffffffffu && mycnt <= (unsigned)CAP) atomicAdd(&cm[(rk[j] >> 16) * G + mych], 1u);
  }
  __syncthreads();

#pragma unroll
  for (int s = 0; s < 3; ++s) {
    const int nl = wave + 16 * s;
    if (nl < NPB) {
      const int c = (int)cm[nl * G + lane];
      const int incl = wave_incl_scan(c, lane);
      cm[nl * G + lane] = (unsigned)(incl - c);
      degs[nl] = __builtin_amdgcn_readlane(incl, 63);
    }
  }
  __syncthreads();

  const int mydeg = lane < NPB ? degs[lane] : 0;
  const int pincl = wave_incl_scan(mydeg, lane);
  const int rp = pincl - mydeg;
  const int T = __builtin_amdgcn_readlane(pincl, 63);
  const bool fast = (T <= LCAP) && (__builtin_amdgcn_readfirstlane(*flag) == 0);

  if (fast) {
    if (wave == 1) rps[lane] = rp;
    {
      unsigned mx = mycnt;
#pragma unroll
      for (int o = 16; o < 64; o <<= 1) mx = max(mx, (unsigned)__shfl_xor((int)mx, o, 64));
      const int nrow = __builtin_amdgcn_readfirstlane((int)((mx + 15u) >> 4));
      unsigned rank[4] = {0u, 0u, 0u, 0u};
      const int nq = __builtin_amdgcn_readfirstlane((int)((mx + 3u) >> 2));
      for (int s4 = 0; s4 < nq; ++s4) {
        const uint4 kq = *reinterpret_cast<const uint4*>(keys + mych * CAP + s4 * 4);
#pragma unroll
        for (int j = 0; j < 4; ++j) {
          if (j < nrow) {
            const unsigned kb = rk[j] & 0xffff0000u, kl = rk[j] & 0xffffu;
            rank[j] += (unsigned)((kq.x - kb) < kl) + (unsigned)((kq.y - kb) < kl) +
                       (unsigned)((kq.z - kb) < kl) + (unsigned)((kq.w - kb) < kl);
          }
        }
      }
#pragma unroll
      for (int j = 0; j < 4; ++j) {
        if (j < nrow) {
          const unsigned nib = min(rk[j] >> 16, (unsigned)(NPB - 1));
          const int base = __shfl(rp, (int)nib, 64);
          if (rk[j] != 0xffffffffu) L[base + cm[nib * G + mych] + rank[j]] = rv[j];
        }
      }
    }
#pragma unroll
    for (int s = 0; s < 3; ++s) {
      const int nl = wave + 16 * s;
      if (nl < NPB) {
        const int dg = __builtin_amdgcn_readlane(mydeg, nl);
        if (dg == 0) *reinterpret_cast<float4*>(QS + nl * QS_STRIDE + lane * 16) = make_float4(0.f, 0.f, 0.f, 0.f);
      }
    }
    if (wave == 0 && lane < 20) L[T + lane] = NULL_REC;
    __syncthreads();

    const int TG = (T + 3) >> 2;
    const int G0 = (wave * TG) >> 4;
    const int G1 = ((wave + 1) * TG) >> 4;
    if (G0 < G1) {
      int ni = __builtin_popcountll(__ballot(lane < NPB && rp <= G0 * 4)) - 1;
      bool mid = __builtin_amdgcn_readlane(rp, ni) < G0 * 4;
      int ende = ni + 1 < NPB ? __builtin_amdgcn_readlane(rp, ni + 1) : 0x7fffffff;
      if (ende >= T) ende = 0x7fffffff;
      float4 q = load_q(Qs + ni * (HH * 2), lane);
      Acc acc = {0.f, 0.f, 0.f, 0.f};
      const unsigned loff = (unsigned)lane * 8u;
      const __amdgpu_buffer_rsrc_t prsrc = __builtin_amdgcn_make_buffer_rsrc(
          const_cast<unsigned short*>(Pp), (short)0, (int)((NN + 1) * HH * 2), 0x00020000);
      unsigned rcur = L[G0 * 4 + lane];
      unsigned rnxt = L[min(G0 * 4 + 64 + lane, LCAP + 31)];

      auto flush = [&]() {
        float* dstrow = mid ? (car + wave * HH) : reinterpret_cast<float*>(QS + ni * QS_STRIDE);
        *reinterpret_cast<float4*>(dstrow + lane * 4) = make_float4(acc.a0, acc.a1, acc.a2, acc.a3);
        if (mid) cnode[wave] = ni;
        mid = false;
        acc.a0 = 0.f; acc.a1 = 0.f; acc.a2 = 0.f; acc.a3 = 0.f;
      };
      auto issue = [&](Grp& b, int g) {
        const int rel = (g - G0) * 4;
        if ((rel & 63) == 0 && rel > 0) {
          rcur = rnxt;
          rnxt = L[min(G0 * 4 + rel + 64 + lane, LCAP + 31)];
        }
        b.r0 = __builtin_amdgcn_readlane(rcur, (rel & 63) + 0);
        b.r1 = __builtin_amdgcn_readlane(rcur, (rel & 63) + 1);
        b.r2 = __builtin_amdgcn_readlane(rcur, (rel & 63) + 2);
        b.r3 = __builtin_amdgcn_readlane(rcur, (rel & 63) + 3);
        b.v0 = gather_row(prsrc, loff, b.r0);
        b.v1 = gather_row(prsrc, loff, b.r1);
        b.v2 = gather_row(prsrc, loff, b.r2);
        b.v3 = gather_row(prsrc, loff, b.r3);
      };
      auto boundary = [&](int e) {
        if (e == ende) {
          flush();
          do {
            ++ni;
            ende = __builtin_amdgcn_readfirstlane(ni + 1 < NPB ? rps[ni + 1] : T);
          } while (e == ende);
          if (ende >= T) ende = 0x7fffffff;
          q = load_q(Qs + ni * (HH * 2), lane);
        }
      };
      auto compute = [&](const Grp& b, int g) {
        boundary(g * 4 + 0); edge_acc(acc, b.r0, b.v0, q, wq0, wq1, wq2, wq3);
        boundary(g * 4 + 1); edge_acc(acc, b.r1, b.v1, q, wq0, wq1, wq2, wq3);
        boundary(g * 4 + 2); edge_acc(acc, b.r2, b.v2, q, wq0, wq1, wq2, wq3);
        boundary(g * 4 + 3); edge_acc(acc, b.r3, b.v3, q, wq0, wq1, wq2, wq3);
      };

      Grp A, B, C;
      issue(A, G0);
      __builtin_amdgcn_sched_barrier(0);
      issue(B, G0 + 1);
      __builtin_amdgcn_sched_barrier(0);
      for (int g = G0; g < G1; g += 3) {
        issue(C, g + 2); compute(A, g);
        issue(A, g + 3); if (g + 1 < G1) compute(B, g + 1);
        issue(B, g + 4); if (g + 2 < G1) compute(C, g + 2);
      }
      flush();
    }
  } else {
#pragma unroll 1
    for (int s = 0; s < 3; ++s) {
      const int nl = wave + 16 * s;
      if (nl < NPB) {
        const float4 q = load_q(Qs + nl * (HH * 2), lane);
        Acc acc = {0.f, 0.f, 0.f, 0.f};
        int total = 0;
        for (int e0 = 0; e0 < EE; e0 += 64) {
          const bool hit = dst[e0 + lane] == n0 + nl;
          unsigned long long m = __ballot(hit);
          if (m != 0ull) {
            const unsigned rec = hit ? (((unsigned)src[e0 + lane] & 0xffffu) | ((unsigned)eattr[e0 + lane] << 16)) : NULL_REC;
            total += __builtin_popcountll(m);
            while (m != 0ull) {
              const int i = __builtin_ctzll(m);
              m &= m - 1ull;
              const unsigned rr = __builtin_amdgcn_readlane(rec, i);
              const uint2 v = *reinterpret_cast<const uint2*>(Pp + (size_t)(rr & 0xffffu) * HH + lane * 4);
              edge_acc(acc, rr, v, q, wq0, wq1, wq2, wq3);
            }
          }
        }
        *reinterpret_cast<float4*>(QS + nl * QS_STRIDE + lane * 16) = make_float4(acc.a0, acc.a1, acc.a2, acc.a3);
        degs[nl] = total;
      }
    }
  }

  const int oct = wave & 7;
  bf16x8 wf[8];
#pragma unroll
  for (int ks = 0; ks < 8; ++ks)
    wf[ks] = *reinterpret_cast<const bf16x8*>(w2T + (oct * 16 + (lane & 15)) * HH + ks * 32 + (lane >> 4) * 8);
  __syncthreads();

#pragma unroll
  for (int s = 0; s < 3; ++s) {
    const int nl = wave + 16 * s;
    if (nl < NPB) {
      unsigned char* row = QS + nl * QS_STRIDE;
      float4 v = *reinterpret_cast<const float4*>(row + lane * 16);
      unsigned long long cm_ = __ballot(lane < 16 && cnode[lane & 15] == nl);
      while (cm_ != 0ull) {
        const int w = __builtin_ctzll(cm_);
        cm_ &= cm_ - 1ull;
        const float4 c = *reinterpret_cast<const float4*>(car + w * HH + lane * 4);
        v.x += c.x; v.y += c.y; v.z += c.z; v.w += c.w;
      }
      const float s0 = v.x * NEG_LN2, s1 = v.y * NEG_LN2, s2 = v.z * NEG_LN2, s3 = v.w * NEG_LN2;
      const unsigned short h0 = f2bf(s0), h1 = f2bf(s1), h2 = f2bf(s2), h3 = f2bf(s3);
      uint2 hv, lv;
      hv.x = (unsigned)h0 | ((unsigned)h1 << 16);
      hv.y = (unsigned)h2 | ((unsigned)h3 << 16);
      lv.x = pack2(s0 - bf2f(h0), s1 - bf2f(h1));
      lv.y = pack2(s2 - bf2f(h2), s3 - bf2f(h3));
      __builtin_amdgcn_wave_barrier();
      *reinterpret_cast<uint2*>(row + lane * 8) = hv;
      *reinterpret_cast<uint2*>(row + 512 + lane * 8) = lv;
    }
  }
  __syncthreads();

  for (int ntile = wave >> 3; ntile < 3; ntile += 2) {
    f32x4 acc = {0.f, 0.f, 0.f, 0.f};
    const int srow = ntile * 16 + (lane & 15);
#pragma unroll
    for (int ks = 0; ks < 8; ++ks) {
      const bf16x8 bh = *reinterpret_cast<const bf16x8*>(QS + srow * QS_STRIDE + ks * 64 + (lane >> 4) * 16);
      const bf16x8 bl = *reinterpret_cast<const bf16x8*>(QS + srow * QS_STRIDE + 512 + ks * 64 + (lane >> 4) * 16);
      acc = __builtin_amdgcn_mfma_f32_16x16x32_bf16(wf[ks], bh, acc, 0, 0, 0);
      acc = __builtin_amdgcn_mfma_f32_16x16x32_bf16(wf[ks], bl, acc, 0, 0, 0);
    }
    if (srow < NPB) {
      const int oc = oct * 16 + (lane >> 4) * 4;
      const float dg = (float)degs[srow];
      uint2 o;
      o.x = pack2(acc[0] + dg * bf2f(b2[oc + 0]), acc[1] + dg * bf2f(b2[oc + 1]));
      o.y = pack2(acc[2] + dg * bf2f(b2[oc + 2]), acc[3] + dg * bf2f(b2[oc + 3]));
      *reinterpret_cast<uint2*>(out + (size_t)(n0 + srow) * DD + oc) = o;
    }
  }
}

}

extern "C" void kernel_launch(void* const* d_in, const int* in_sizes, int n_in,
                              void* d_out, int out_size, void* d_ws, size_t ws_size,
                              hipStream_t stream) {
  const unsigned short* x  = (const unsigned short*)d_in[0];
  const unsigned short* ea = (const unsigned short*)d_in[1];
  const int* src = (const int*)d_in[2];
  const int* dst = (const int*)d_in[3];
  const unsigned short* w1 = (const unsigned short*)d_in[4];
  const unsigned short* b1 = (const unsigned short*)d_in[5];
  const unsigned short* w2 = (const unsigned short*)d_in[6];
  const unsigned short* b2 = (const unsigned short*)d_in[7];
  unsigned char* ws = (unsigned char*)d_ws;
  unsigned short* Pp = (unsigned short*)(ws + WS_P);
  unsigned short* Qp = (unsigned short*)(ws + WS_Q);
  uint2* bk = (uint2*)(ws + WS_BK);
  unsigned* cntg = (unsigned*)(ws + WS_CNT);
  unsigned short* w2T = (unsigned short*)(ws + WS_W2T);

  k1_prep<<<K1_BLOCKS, 1024, 0, stream>>>(x, ea, src, dst, w1, b1, w2, Pp, Qp, bk, cntg, w2T);
  k2_agg<<<NB, 1024, 0, stream>>>(Pp, Qp, bk, cntg, w2T, w1, b2, ea, src, dst, (unsigned short*)d_out);
}
```

```cpp
#include <hip/hip_runtime.h>
#include <hip/hip_bf16.h>
#include <stdint.h>

using bf16x8 = __attribute__((ext_vector_type(8))) short;
using s16x4  = __attribute__((ext_vector_type(4))) short;
using f32x4  = __attribute__((ext_vector_type(4))) float;
typedef __attribute__((address_space(3))) s16x4* lds_s16x4_ptr;

namespace {

constexpr int NN  = 10000;
constexpr int EE  = 320000;
constexpr int DD  = 128;
constexpr int HH  = 256;
constexpr int G   = 64;
constexpr int CH  = EE / G;
constexpr int NPB = 40;
constexpr int NB  = NN / NPB;
constexpr int CAP = 64;
constexpr int GEMM_BLOCKS = 160;
constexpr int K1_BLOCKS = G + GEMM_BLOCKS;
constexpr int W_STRIDE = 288;
constexpr int O_STRIDE = 272;

constexpr size_t WS_P    = 0;
constexpr size_t WS_Q    = WS_P + (size_t)(NN + 1) * HH * 2;
constexpr size_t WS_BK   = WS_Q + (size_t)NN * HH * 2;
constexpr size_t WS_CNT  = WS_BK + (size_t)NB * G * CAP * 8;
constexpr size_t WS_W2T  = WS_CNT + (size_t)NB * G * 4;
constexpr size_t WS_END  = WS_W2T + (size_t)DD * HH * 2;

constexpr float NEG_LOG2E = -1.4426950408889634f;
constexpr float NEG_LN2   = -0.6931471805599453f;
constexpr unsigned NULL_REC = (unsigned)NN;
constexpr unsigned short NULL_VAL = 0x7149;

__device__ __forceinline__ float bf2f(unsigned short u) { return __uint_as_float(((unsigned)u) << 16); }
__device__ __forceinline__ unsigned short f2bf(float f) {
  __hip_bfloat16 h = __float2bfloat16(f);
  return *reinterpret_cast<unsigned short*>(&h);
}
__device__ __forceinline__ unsigned pack2(float a, float b) {
  return (unsigned)f2bf(a) | ((unsigned)f2bf(b) << 16);
}

constexpr int K1_W = 0;
constexpr int K1_O = DD * W_STRIDE;
constexpr int K1_LDS = K1_O + 8 * 16 * O_STRIDE;

__global__ __launch_bounds__(1024) void k1_prep(
    const unsigned short* __restrict__ x, const unsigned short* __restrict__ eattr,
    const int* __restrict__ src, const int* __restrict__ dst,
    const unsigned short* __restrict__ w1, const unsigned short* __restrict__ b1,
    const unsigned short* __restrict__ w2,
    unsigned short* __restrict__ Pp, unsigned short* __restrict__ Qp,
    uint2* __restrict__ bk, unsigned* __restrict__ cntg,
    unsigned short* __restrict__ w2T) {
  __shared__ __attribute__((aligned(16))) unsigned char smem[K1_LDS];
  const int tid = threadIdx.x;
  const int lane = tid & 63;
  const int wave = tid >> 6;
  const int bid = blockIdx.x;

  if (bid < G) {
    unsigned* hist = reinterpret_cast<unsigned*>(smem);
    const int ebase = bid * CH;
    int d[5];
    unsigned rec[5];
#pragma unroll
    for (int i = 0; i < 5; ++i) {
      const int idx = i * 1024 + tid;
      d[i] = 0; rec[i] = 0;
      if (idx < CH) {
        d[i] = dst[ebase + idx];
        rec[i] = ((unsigned)src[ebase + idx] & 0xffffu) | ((unsigned)eattr[ebase + idx] << 16);
      }
    }
    if (tid < 256) hist[tid] = 0u;
    __syncthreads();
#pragma unroll
    for (int i = 0; i < 5; ++i) {
      const int idx = i * 1024 + tid;
      if (idx < CH) {
        const int bin = d[i] / NPB;
        const int nib = d[i] - bin * NPB;
        const unsigned slot = atomicAdd(&hist[bin], 1u);
        if (slot < CAP) bk[((size_t)bin * G + bid) * CAP + slot] = make_uint2(((unsigned)nib << 16) | (unsigned)idx, rec[i]);
      }
    }
    __syncthreads();
    if (tid < NB) cntg[tid * G + bid] = hist[tid];
  } else {
    const int gb = bid - G;
    const int half = gb / 80;
    const int hh = (gb / 40) & 1;
    const int tg = gb % 40;
#pragma unroll
    for (int i = 0; i < 2; ++i) {
      const int k = (tid >> 4) + 64 * i;
      const int c8 = (tid & 15) * 8;
      const uint4 v = *reinterpret_cast<const uint4*>(w1 + (size_t)(half * DD + k) * HH + hh * 128 + c8);
      *reinterpret_cast<uint4*>(smem + K1_W + k * W_STRIDE + c8 * 2) = v;
    }
    if (gb < 64 && tid >= 512) {
      const int i = gb * 512 + tid - 512;
      const int k = i >> 7, oc = i & 127;
      w2T[oc * HH + k] = w2[i];
    }
    if (gb == 64 && tid >= 512 && tid < 512 + HH) Pp[(size_t)NN * HH + tid - 512] = NULL_VAL;
    const int nt0 = tg * 16 + wave * 2;
    const bool active = wave < 8 && nt0 < NN / 16;
    const int ntc0 = nt0 < NN / 16 ? nt0 : NN / 16 - 1;
    const int ntc1 = nt0 + 1 < NN / 16 ? nt0 + 1 : NN / 16 - 1;
    bf16x8 xb0[4], xb1[4];
    uint2 bv[8];
    if (active) {
#pragma unroll
      for (int ks = 0; ks < 4; ++ks) {
        xb0[ks] = *reinterpret_cast<const bf16x8*>(x + (size_t)(ntc0 * 16 + (lane & 15)) * DD + ks * 32 + (lane >> 4) * 8);
        xb1[ks] = *reinterpret_cast<const bf16x8*>(x + (size_t)(ntc1 * 16 + (lane & 15)) * DD + ks * 32 + (lane >> 4) * 8);
      }
#pragma unroll
      for (int ct = 0; ct < 8; ++ct) {
        bv[ct] = *reinterpret_cast<const uint2*>(b1 + hh * 128 + ct * 16 + (lane >> 4) * 4);
        if (!half) bv[ct] = make_uint2(0u, 0u);
      }
    }
    __syncthreads();
    if (active) {
      unsigned char* ob = smem + K1_O + wave * (16 * O_STRIDE);
      const unsigned lbase = (unsigned)(size_t)smem + K1_W + ((lane >> 4) * 8 + ((lane & 15) >> 2)) * W_STRIDE + (lane & 3) * 8;
      uint2 o1[8];
#pragma unroll
      for (int ct = 0; ct < 8; ++ct) {
        f32x4 acc0 = {__uint_as_float(bv[ct].x << 16), __uint_as_float(bv[ct].x & 0xffff0000u),
                      __uint_as_float(bv[ct].y << 16), __uint_as_float(bv[ct].y & 0xffff0000u)};
        f32x4 acc1 = acc0;
#pragma unroll
        for (int ks = 0; ks < 4; ++ks) {
          const unsigned a0 = lbase + (ks * 32) * W_STRIDE + ct * 32;
          const s16x4 lo = __builtin_amdgcn_ds_read_tr16_b64_v4i16((lds_s16x4_ptr)(size_t)a0);
          const s16x4 hi = __builtin_amdgcn_ds_read_tr16_b64_v4i16((lds_s16x4_ptr)(size_t)(a0 + 4 * W_STRIDE));
          const bf16x8 a = {lo[0], lo[1], lo[2], lo[3], hi[0], hi[1], hi[2], hi[3]};
          acc0 = __builtin_amdgcn_mfma_f32_16x16x32_bf16(a, xb0[ks], acc0, 0, 0, 0);
          acc1 = __builtin_amdgcn_mfma_f32_16x16x32_bf16(a, xb1[ks], acc1, 0, 0, 0);
        }
        uint2 o;
        o.x = pack2(acc0[0] * NEG_LOG2E, acc0[1] * NEG_LOG2E);
        o.y = pack2(acc0[2] * NEG_LOG2E, acc0[3] * NEG_LOG2E);
        *reinterpret_cast<uint2*>(ob + (lane & 15) * O_STRIDE + ct * 32 + (lane >> 4) * 8) = o;
        o1[ct].x = pack2(acc1[0] * NEG_LOG2E, acc1[1] * NEG_LOG2E);
        o1[ct].y = pack2(acc1[2] * NEG_LOG2E, acc1[3] * NEG_LOG2E);
      }
      unsigned short* dstp = (half ? Qp : Pp) + (size_t)(nt0 * 16) * HH + hh * 128;
#pragma unroll
      for (int r = 0; r < 4; ++r) {
        const int row = r * 4 + (lane >> 4);
        const uint4 v = *reinterpret_cast<const uint4*>(ob + row * O_STRIDE + (lane & 15) * 16);
        *reinterpret_cast<uint4*>(dstp + (size_t)row * HH + (lane & 15) * 8) = v;
      }
      if (nt0 + 1 < NN / 16) {
        __builtin_amdgcn_wave_barrier();
#pragma unroll
        for (int ct = 0; ct < 8; ++ct)
          *reinterpret_cast<uint2*>(ob + (lane & 15) * O_STRIDE + ct * 32 + (lane >> 4) * 8) = o1[ct];
#pragma unroll
        for (int r = 0; r < 4; ++r) {
          const int row = r * 4 + (lane >> 4);
          const uint4 v = *reinterpret_cast<const uint4*>(ob + row * O_STRIDE + (lane & 15) * 16);
          *reinterpret_cast<uint4*>(dstp + (size_t)(16 + row) * HH + (lane & 15) * 8) = v;
        }
      }
    }
  }
}

constexpr int QS_STRIDE = 1040;
constexpr int LCAP = 4096;
constexpr int K2_QS   = 0;
constexpr int K2_Q    = K2_QS + 48 * QS_STRIDE;
constexpr int K2_CAR  = K2_Q + NPB * HH * 2;
constexpr int K2_KEY  = K2_CAR + 16 * HH * 4;
constexpr int K2_CM   = K2_KEY + G * CAP * 4;
constexpr int K2_L    = K2_CM + NPB * G * 4;
constexpr int K2_LE   = K2_L + (LCAP + 32) * 4;
constexpr int K2_DEG  = K2_LE + (LCAP + 32) * 4;
constexpr int K2_RP   = K2_DEG + 64 * 4;
constexpr int K2_CN   = K2_RP + 64 * 4;
constexpr int K2_BC   = K2_CN + 16 * 4;
constexpr int K2_FLAG = K2_BC + 64 * 4;
constexpr int K2_LDS  = K2_FLAG + 16;

__device__ __forceinline__ float sig_t(float t) {
  return __builtin_amdgcn_rcpf(1.0f + __builtin_amdgcn_exp2f(t));
}

struct Acc { float a0, a1, a2, a3; };

__device__ __forceinline__ void edge_acc_e(Acc& a, float e, uint2 v, const float4& q,
                                           float wq0, float wq1, float wq2, float wq3) {
  const float t0 = fmaf(e, wq0, q.x) + __uint_as_float(v.x << 16);
  const float t1 = fmaf(e, wq1, q.y) + __uint_as_float(v.x & 0xffff0000u);
  const float t2 = fmaf(e, wq2, q.z) + __uint_as_float(v.y << 16);
  const float t3 = fmaf(e, wq3, q.w) + __uint_as_float(v.y & 0xffff0000u);
  a.a0 = fmaf(t0, sig_t(t0), a.a0);
  a.a1 = fmaf(t1, sig_t(t1), a.a1);
  a.a2 = fmaf(t2, sig_t(t2), a.a2);
  a.a3 = fmaf(t3, sig_t(t3), a.a3);
}

__device__ __forceinline__ void edge_acc(Acc& a, unsigned rec, uint2 v, const float4& q,
                                         float wq0, float wq1, float wq2, float wq3) {
  edge_acc_e(a, __uint_as_float(rec & 0xffff0000u), v, q, wq0, wq1, wq2, wq3);
}

__device__ __forceinline__ float4 load_q(const unsigned char* qrow, int lane) {
  const uint2 v = *reinterpret_cast<const uint2*>(qrow + lane * 8);
  return make_float4(__uint_as_float(v.x << 16), __uint_as_float(v.x & 0xffff0000u),
                     __uint_as_float(v.y << 16), __uint_as_float(v.y & 0xffff0000u));
}

__device__ __forceinline__ int wave_incl_scan(int v, int lane) {
#pragma unroll
  for (int o = 1; o < 64; o <<= 1) {
    const int t = __shfl_up(v, o, 64);
    if (lane >= o) v += t;
  }
  return v;
}

struct Grp { unsigned r0, r1, r2, r3; uint2 v0, v1, v2, v3; };

__device__ __forceinline__ uint2 gather_off(__amdgpu_buffer_rsrc_t rsrc, unsigned loff, unsigned roff) {
  typedef unsigned u32x2v __attribute__((ext_vector_type(2)));
  const u32x2v v = __builtin_amdgcn_raw_buffer_load_b64(rsrc, (int)loff, (int)roff, 0);
  return make_uint2(v[0], v[1]);
}

__global__ __launch_bounds__(1024) void k2_agg(
    const unsigned short* __restrict__ Pp, const unsigned short* __restrict__ Qp,
    const uint2* __restrict__ bk, const unsigned* __restrict__ cntg,
    const unsigned short* __restrict__ w2T, const unsigned short* __restrict__ w1,
    const unsigned short* __restrict__ b2,
    const unsigned short* __restrict__ eattr, const int* __restrict__ src, const int* __restrict__ dst,
    unsigned short* __restrict__ out) {
  __shared__ __attribute__((aligned(16))) unsigned char smem[K2_LDS];
  unsigned char* QS = smem + K2_QS;
  unsigned char* Qs = smem + K2_Q;
  float* car = reinterpret_cast<float*>(smem + K2_CAR);
  unsigned* keys = reinterpret_cast<unsigned*>(smem + K2_KEY);
  unsigned* cm = reinterpret_cast<unsigned*>(smem + K2_CM);
  unsigned* L = reinterpret_cast<unsigned*>(smem + K2_L);
  unsigned* LE = reinterpret_cast<unsigned*>(smem + K2_LE);
  int* degs = reinterpret_cast<int*>(smem + K2_DEG);
  int* rps = reinterpret_cast<int*>(smem + K2_RP);
  int* cnode = reinterpret_cast<int*>(smem + K2_CN);
  unsigned* bcnt = reinterpret_cast<unsigned*>(smem + K2_BC);
  int* flag = reinterpret_cast<int*>(smem + K2_FLAG);

  const int tid = threadIdx.x;
  const int lane = tid & 63;
  const int wave = __builtin_amdgcn_readfirstlane(tid >> 6);
  const int n0 = blockIdx.x * NPB;

  const int mych = tid >> 4;
  const int mysl = tid & 15;
  const unsigned mycnt = cntg[blockIdx.x * G + mych];
  uint2 rr4[4];
  {
    const uint2* bp = bk + ((size_t)blockIdx.x * G + mych) * CAP + mysl;
#pragma unroll
    for (int j = 0; j < 4; ++j) rr4[j] = bp[16 * j];
  }
  {
    const uint4* qg = reinterpret_cast<const uint4*>(Qp + (size_t)n0 * HH);
    for (int i = tid; i < NPB * HH / 8; i += 1024) reinterpret_cast<uint4*>(Qs)[i] = qg[i];
  }
  float wq0, wq1, wq2, wq3;
  {
    const uint2 wv = *reinterpret_cast<const uint2*>(w1 + (size_t)(2 * DD) * HH + lane * 4);
    wq0 = __uint_as_float(wv.x << 16) * NEG_LOG2E;
    wq1 = __uint_as_float(wv.x & 0xffff0000u) * NEG_LOG2E;
    wq2 = __uint_as_float(wv.y << 16) * NEG_LOG2E;
    wq3 = __uint_as_float(wv.y & 0xffff0000u) * NEG_LOG2E;
  }
  for (int i = tid; i < NPB * G; i += 1024) cm[i] = 0u;
  if (tid == 0) *flag = 0;
  if (tid < 16) cnode[tid] = -1;
  __syncthreads();

  unsigned rk[4], rv[4];
#pragma unroll
  for (int j = 0; j < 4; ++j) {
    const bool ok = (unsigned)(mysl + 16 * j) < mycnt;
    rk[j] = ok ? rr4[j].x : 0xffffffffu;
    rv[j] = rr4[j].y;
  }
  if (mycnt > (unsigned)CAP) *flag = 1;
#pragma unroll
  for (int j = 0; j < 4; ++j) {
    keys[mych * CAP + mysl + 16 * j] = rk[j];
    if (rk[j] != 0xffffffffu && mycnt <= (unsigned)CAP) atomicAdd(&cm[(rk[j] >> 16) * G + mych], 1u);
  }
  __syncthreads();

#pragma unroll
  for (int s = 0; s < 3; ++s) {
    const int nl = wave + 16 * s;
    if (nl < NPB) {
      const int c = (int)cm[nl * G + lane];
      const int incl = wave_incl_scan(c, lane);
      cm[nl * G + lane] = (unsigned)(incl - c);
      degs[nl] = __builtin_amdgcn_readlane(incl, 63);
    }
  }
  __syncthreads();

  const int mydeg = lane < NPB ? degs[lane] : 0;
  const int pincl = wave_incl_scan(mydeg, lane);
  const int rp = pincl - mydeg;
  const int T = __builtin_amdgcn_readlane(pincl, 63);
  const bool fast = (T <= LCAP) && (__builtin_amdgcn_readfirstlane(*flag) == 0);

  if (fast) {
    if (wave == 1) rps[lane] = rp;
    {
      unsigned mx = mycnt;
#pragma unroll
      for (int o = 16; o < 64; o <<= 1) mx = max(mx, (unsigned)__shfl_xor((int)mx, o, 64));
      const int nrow = __builtin_amdgcn_readfirstlane((int)((mx + 15u) >> 4));
      unsigned rank[4] = {0u, 0u, 0u, 0u};
      const int nq = __builtin_amdgcn_readfirstlane((int)((mx + 3u) >> 2));
      for (int s4 = 0; s4 < nq; ++s4) {
        const uint4 kq = *reinterpret_cast<const uint4*>(keys + mych * CAP + s4 * 4);
#pragma unroll
        for (int j = 0; j < 4; ++j) {
          if (j < nrow) {
            const unsigned kb = rk[j] & 0xffff0000u, kl = rk[j] & 0xffffu;
            rank[j] += (unsigned)((kq.x - kb) < kl) + (unsigned)((kq.y - kb) < kl) +
                       (unsigned)((kq.z - kb) < kl) + (unsigned)((kq.w - kb) < kl);
          }
        }
      }
#pragma unroll
      for (int j = 0; j < 4; ++j) {
        if (j < nrow) {
          const unsigned nib = min(rk[j] >> 16, (unsigned)(NPB - 1));
          const int base = __shfl(rp, (int)nib, 64);
          if (rk[j] != 0xffffffffu) {
            const int pos = base + cm[nib * G + mych] + rank[j];
            L[pos] = (rv[j] & 0xffffu) * (HH * 2);
            LE[pos] = rv[j] & 0xffff0000u;
          }
        }
      }
    }
#pragma unroll
    for (int s = 0; s < 3; ++s) {
      const int nl = wave + 16 * s;
      if (nl < NPB) {
        const int dg = __builtin_amdgcn_readlane(mydeg, nl);
        if (dg == 0) *reinterpret_cast<float4*>(QS + nl * QS_STRIDE + lane * 16) = make_float4(0.f, 0.f, 0.f, 0.f);
      }
    }
    if (wave == 0 && lane < 20) { L[T + lane] = NULL_REC * (HH * 2); LE[T + lane] = 0u; }
    __syncthreads();

    const int TG = (T + 3) >> 2;
    const int G0 = (wave * TG) >> 4;
    const int G1 = ((wave + 1) * TG) >> 4;
    if (G0 < G1) {
      int ni = __builtin_popcountll(__ballot(lane < NPB && rp <= G0 * 4)) - 1;
      bool mid = __builtin_amdgcn_readlane(rp, ni) < G0 * 4;
      int ende = ni + 1 < NPB ? __builtin_amdgcn_readlane(rp, ni + 1) : 0x7fffffff;
      if (ende >= T) ende = 0x7fffffff;
      float4 q = load_q(Qs + ni * (HH * 2), lane);
      Acc acc = {0.f, 0.f, 0.f, 0.f};
      const unsigned loff = (unsigned)lane * 8u;
      const __amdgpu_buffer_rsrc_t prsrc = __builtin_amdgcn_make_buffer_rsrc(
          const_cast<unsigned short*>(Pp), (short)0, (int)((NN + 1) * HH * 2), 0x00020000);
      unsigned rcur = L[G0 * 4 + lane], ecur = LE[G0 * 4 + lane];
      unsigned rnxt = L[min(G0 * 4 + 64 + lane, LCAP + 31)], enxt = LE[min(G0 * 4 + 64 + lane, LCAP + 31)];

      auto flush = [&]() {
        float* dstrow = mid ? (car + wave * HH) : reinterpret_cast<float*>(QS + ni * QS_STRIDE);
        *reinterpret_cast<float4*>(dstrow + lane * 4) = make_float4(acc.a0, acc.a1, acc.a2, acc.a3);
        if (mid) cnode[wave] = ni;
        mid = false;
        acc.a0 = 0.f; acc.a1 = 0.f; acc.a2 = 0.f; acc.a3 = 0.f;
      };
      auto issue = [&](Grp& b, int g) {
        const int rel = (g - G0) * 4;
        if ((rel & 63) == 0 && rel > 0) {
          rcur = rnxt; ecur = enxt;
          rnxt = L[min(G0 * 4 + rel + 64 + lane, LCAP + 31)];
          enxt = LE[min(G0 * 4 + rel + 64 + lane, LCAP + 31)];
        }
        b.r0 = __builtin_amdgcn_readlane(ecur, (rel & 63) + 0);
        b.r1 = __builtin_amdgcn_readlane(ecur, (rel & 63) + 1);
        b.r2 = __builtin_amdgcn_readlane(ecur, (rel & 63) + 2);
        b.r3 = __builtin_amdgcn_readlane(ecur, (rel & 63) + 3);
        b.v0 = gather_off(prsrc, loff, __builtin_amdgcn_readlane(rcur, (rel & 63) + 0));
        b.v1 = gather_off(prsrc, loff, __builtin_amdgcn_readlane(rcur, (rel & 63) + 1));
        b.v2 = gather_off(prsrc, loff, __builtin_amdgcn_readlane(rcur, (rel & 63) + 2));
        b.v3 = gather_off(prsrc, loff, __builtin_amdgcn_readlane(rcur, (rel & 63) + 3));
      };
      auto boundary = [&](int e) {
        if (e == ende) {
          flush();
          do {
            ++ni;
            ende = __builtin_amdgcn_readfirstlane(ni + 1 < NPB ? rps[ni + 1] : T);
          } while (e == ende);
          if (ende >= T) ende = 0x7fffffff;
          q = load_q(Qs + ni * (HH * 2), lane);
        }
      };
      auto compute = [&](const Grp& b, int g) {
        boundary(g * 4 + 0); edge_acc_e(acc, __uint_as_float(b.r0), b.v0, q, wq0, wq1, wq2, wq3);
        boundary(g * 4 + 1); edge_acc_e(acc, __uint_as_float(b.r1), b.v1, q, wq0, wq1, wq2, wq3);
        boundary(g * 4 + 2); edge_acc_e(acc, __uint_as_float(b.r2), b.v2, q, wq0, wq1, wq2, wq3);
        boundary(g * 4 + 3); edge_acc_e(acc, __uint_as_float(b.r3), b.v3, q, wq0, wq1, wq2, wq3);
      };

      Grp A, B, C;
      issue(A, G0);
      __builtin_amdgcn_sched_barrier(0);
      issue(B, G0 + 1);
      __builtin_amdgcn_sched_barrier(0);
      for (int g = G0; g < G1; g += 3) {
        issue(C, g + 2); compute(A, g);
        issue(A, g + 3); if (g + 1 < G1) compute(B, g + 1);
        issue(B, g + 4); if (g + 2 < G1) compute(C, g + 2);
      }
      flush();
    }
  } else {
#pragma unroll 1
    for (int s = 0; s < 3; ++s) {
      const int nl = wave + 16 * s;
      if (nl < NPB) {
        const float4 q = load_q(Qs + nl * (HH * 2), lane);
        Acc acc = {0.f, 0.f, 0.f, 0.f};
        int total = 0;
        for (int e0 = 0; e0 < EE; e0 += 64) {
          const bool hit = dst[e0 + lane] == n0 + nl;
          unsigned long long m = __ballot(hit);
          if (m != 0ull) {
            const unsigned rec = hit ? (((unsigned)src[e0 + lane] & 0xffffu) | ((unsigned)eattr[e0 + lane] << 16)) : NULL_REC;
            total += __builtin_popcountll(m);
            while (m != 0ull) {
              const int i = __builtin_ctzll(m);
              m &= m - 1ull;
              const unsigned rr = __builtin_amdgcn_readlane(rec, i);
              const uint2 v = *reinterpret_cast<const uint2*>(Pp + (size_t)(rr & 0xffffu) * HH + lane * 4);
              edge_acc(acc, rr, v, q, wq0, wq1, wq2, wq3);
            }
          }
        }
        *reinterpret_cast<float4*>(QS + nl * QS_STRIDE + lane * 16) = make_float4(acc.a0, acc.a1, acc.a2, acc.a3);
        degs[nl] = total;
      }
    }
  }

  const int oct = wave & 7;
  bf16x8 wf[8];
#pragma unroll
  for (int ks = 0; ks < 8; ++ks)
    wf[ks] = *reinterpret_cast<const bf16x8*>(w2T + (oct * 16 + (lane & 15)) * HH + ks * 32 + (lane >> 4) * 8);
  __syncthreads();

#pragma unroll
  for (int s = 0; s < 3; ++s) {
    const int nl = wave + 16 * s;
    if (nl < NPB) {
      unsigned char* row = QS + nl * QS_STRIDE;
      float4 v = *reinterpret_cast<const float4*>(row + lane * 16);
      unsigned long long cm_ = __ballot(lane < 16 && cnode[lane & 15] == nl);
      while (cm_ != 0ull) {
        const int w = __builtin_ctzll(cm_);
        cm_ &= cm_ - 1ull;
        const float4 c = *reinterpret_cast<const float4*>(car + w * HH + lane * 4);
        v.x += c.x; v.y += c.y; v.z += c.z; v.w += c.w;
      }
      const float s0 = v.x * NEG_LN2, s1 = v.y * NEG_LN2, s2 = v.z * NEG_LN2, s3 = v.w * NEG_LN2;
      const unsigned short h0 = f2bf(s0), h1 = f2bf(s1), h2 = f2bf(s2), h3 = f2bf(s3);
      uint2 hv, lv;
      hv.x = (unsigned)h0 | ((unsigned)h1 << 16);
      hv.y = (unsigned)h2 | ((unsigned)h3 << 16);
      lv.x = pack2(s0 - bf2f(h0), s1 - bf2f(h1));
      lv.y = pack2(s2 - bf2f(h2), s3 - bf2f(h3));
      __builtin_amdgcn_wave_barrier();
      *reinterpret_cast<uint2*>(row + lane * 8) = hv;
      *reinterpret_cast<uint2*>(row + 512 + lane * 8) = lv;
    }
  }
  __syncthreads();

  for (int ntile = wave >> 3; ntile < 3; ntile += 2) {
    f32x4 acc = {0.f, 0.f, 0.f, 0.f};
    const int srow = ntile * 16 + (lane & 15);
#pragma unroll
    for (int ks = 0; ks < 8; ++ks) {
      const bf16x8 bh = *reinterpret_cast<const bf16x8*>(QS + srow * QS_STRIDE + ks * 64 + (lane >> 4) * 16);
      const bf16x8 bl = *reinterpret_cast<const bf16x8*>(QS + srow * QS_STRIDE + 512 + ks * 64 + (lane >> 4) * 16);
      acc = __builtin_amdgcn_mfma_f32_16x16x32_bf16(wf[ks], bh, acc, 0, 0, 0);
      acc = __builtin_amdgcn_mfma_f32_16x16x32_bf16(wf[ks], bl, acc, 0, 0, 0);
    }
    if (srow < NPB) {
      const int oc = oct * 16 + (lane >> 4) * 4;
      const float dg = (float)degs[srow];
      uint2 o;
      o.x = pack2(acc[0] + dg * bf2f(b2[oc + 0]), acc[1] + dg * bf2f(b2[oc + 1]));
      o.y = pack2(acc[2] + dg * bf2f(b2[oc + 2]), acc[3] + dg * bf2f(b2[oc + 3]));
      *reinterpret_cast<uint2*>(out + (size_t)(n0 + srow) * DD + oc) = o;
    }
  }
}

}

extern "C" void kernel_launch(void* const* d_in, const int* in_sizes, int n_in,
                              void* d_out, int out_size, void* d_ws, size_t ws_size,
                              hipStream_t stream) {
  const unsigned short* x  = (const unsigned short*)d_in[0];
  const unsigned short* ea = (const unsigned short*)d_in[1];
  const int* src = (const int*)d_in[2];
  const int* dst = (const int*)d_in[3];
  const unsigned short* w1 = (const unsigned short*)d_in[4];
  const unsigned short* b1 = (const unsigned short*)d_in[5];
  const unsigned short* w2 = (const unsigned short*)d_in[6];
  const unsigned short* b2 = (const unsigned short*)d_in[7];
  unsigned char* ws = (unsigned char*)d_ws;
  unsigned short* Pp = (unsigned short*)(ws + WS_P);
  unsigned short* Qp = (unsigned short*)(ws + WS_Q);
  uint2* bk = (uint2*)(ws + WS_BK);
  unsigned* cntg = (unsigned*)(ws + WS_CNT);
  unsigned short* w2T = (unsigned short*)(ws + WS_W2T);

  k1_prep<<<K1_BLOCKS, 1024, 0, stream>>>(x, ea, src, dst, w1, b1, w2, Pp, Qp, bk, cntg, w2T);
  k2_agg<<<NB, 1024, 0, stream>>>(Pp, Qp, bk, cntg, w2T, w1, b2, ea, src, dst, (unsigned short*)d_out);
}
```

```cpp
#include <hip/hip_runtime.h>
#include <hip/hip_bf16.h>
#include <stdint.h>

using bf16x8 = __attribute__((ext_vector_type(8))) short;
using s16x4  = __attribute__((ext_vector_type(4))) short;
using f32x4  = __attribute__((ext_vector_type(4))) float;
typedef __attribute__((address_space(3))) s16x4* lds_s16x4_ptr;

namespace {

constexpr int NN  = 10000;
constexpr int EE  = 320000;
constexpr int DD  = 128;
constexpr int HH  = 256;
constexpr int G   = 64;
constexpr int CH  = EE / G;
constexpr int NPB = 20;
constexpr int NB  = NN / NPB;
constexpr int CAP = 32;
constexpr int SPT = CAP / 16;
constexpr int NPW = (NPB + 15) / 16;
constexpr int GEMM_BLOCKS = 160;
constexpr int K1_BLOCKS = G + GEMM_BLOCKS;
constexpr int W_STRIDE = 288;
constexpr int O_STRIDE = 272;

constexpr size_t WS_P    = 0;
constexpr size_t WS_Q    = WS_P + (size_t)(NN + 1) * HH * 2;
constexpr size_t WS_BK   = WS_Q + (size_t)NN * HH * 2;
constexpr size_t WS_CNT  = WS_BK + (size_t)NB * G * CAP * 8;
constexpr size_t WS_W2T  = WS_CNT + (size_t)NB * G * 4;
constexpr size_t WS_END  = WS_W2T + (size_t)DD * HH * 2;

constexpr float NEG_LOG2E = -1.4426950408889634f;
constexpr float NEG_LN2   = -0.6931471805599453f;
constexpr unsigned NULL_REC = (unsigned)NN;
constexpr unsigned short NULL_VAL = 0x7149;

__device__ __forceinline__ float bf2f(unsigned short u) { return __uint_as_float(((unsigned)u) << 16); }
__device__ __forceinline__ unsigned short f2bf(float f) {
  __hip_bfloat16 h = __float2bfloat16(f);
  return *reinterpret_cast<unsigned short*>(&h);
}
__device__ __forceinline__ unsigned pack2(float a, float b) {
  return (unsigned)f2bf(a) | ((unsigned)f2bf(b) << 16);
}

constexpr int K1_W = 0;
constexpr int K1_O = DD * W_STRIDE;
constexpr int K1_LDS = K1_O + 8 * 16 * O_STRIDE;

__global__ __launch_bounds__(1024) void k1_prep(
    const unsigned short* __restrict__ x, const unsigned short* __restrict__ eattr,
    const int* __restrict__ src, const int* __restrict__ dst,
    const unsigned short* __restrict__ w1, const unsigned short* __restrict__ b1,
    const unsigned short* __restrict__ w2,
    unsigned short* __restrict__ Pp, unsigned short* __restrict__ Qp,
    uint2* __restrict__ bk, unsigned* __restrict__ cntg,
    unsigned short* __restrict__ w2T) {
  __shared__ __attribute__((aligned(16))) unsigned char smem[K1_LDS];
  const int tid = threadIdx.x;
  const int lane = tid & 63;
  const int wave = tid >> 6;
  const int bid = blockIdx.x;

  if (bid < G) {
    unsigned* hist = reinterpret_cast<unsigned*>(smem);
    const int ebase = bid * CH;
    int d[5];
    unsigned rec[5];
#pragma unroll
    for (int i = 0; i < 5; ++i) {
      const int idx = i * 1024 + tid;
      d[i] = 0; rec[i] = 0;
      if (idx < CH) {
        d[i] = dst[ebase + idx];
        rec[i] = ((unsigned)src[ebase + idx] & 0xffffu) | ((unsigned)eattr[ebase + idx] << 16);
      }
    }
    if (tid < 512) hist[tid] = 0u;
    __syncthreads();
#pragma unroll
    for (int i = 0; i < 5; ++i) {
      const int idx = i * 1024 + tid;
      if (idx < CH) {
        const int bin = d[i] / NPB;
        const int nib = d[i] - bin * NPB;
        const unsigned slot = atomicAdd(&hist[bin], 1u);
        if (slot < CAP) bk[((size_t)bin * G + bid) * CAP + slot] = make_uint2(((unsigned)nib << 16) | (unsigned)idx, rec[i]);
      }
    }
    __syncthreads();
    if (tid < NB) cntg[tid * G + bid] = hist[tid];
  } else {
    const int gb = bid - G;
    const int half = gb / 80;
    const int hh = (gb / 40) & 1;
    const int tg = gb % 40;
#pragma unroll
    for (int i = 0; i < 2; ++i) {
      const int k = (tid >> 4) + 64 * i;
      const int c8 = (tid & 15) * 8;
      const uint4 v = *reinterpret_cast<const uint4*>(w1 + (size_t)(half * DD + k) * HH + hh * 128 + c8);
      *reinterpret_cast<uint4*>(smem + K1_W + k * W_STRIDE + c8 * 2) = v;
    }
    if (gb < 64 && tid >= 512) {
      const int i = gb * 512 + tid - 512;
      const int k = i >> 7, oc = i & 127;
      w2T[oc * HH + k] = w2[i];
    }
    if (gb == 64 && tid >= 512 && tid < 512 + HH) Pp[(size_t)NN * HH + tid - 512] = NULL_VAL;
    const int nt0 = tg * 16 + wave * 2;
    const bool active = wave < 8 && nt0 < NN / 16;
    const int ntc0 = nt0 < NN / 16 ? nt0 : NN / 16 - 1;
    const int ntc1 = nt0 + 1 < NN / 16 ? nt0 + 1 : NN / 16 - 1;
    bf16x8 xb0[4], xb1[4];
    uint2 bv[8];
    if (active) {
#pragma unroll
      for (int ks = 0; ks < 4; ++ks) {
        xb0[ks] = *reinterpret_cast<const bf16x8*>(x + (size_t)(ntc0 * 16 + (lane & 15)) * DD + ks * 32 + (lane >> 4) * 8);
        xb1[ks] = *reinterpret_cast<const bf16x8*>(x + (size_t)(ntc1 * 16 + (lane & 15)) * DD + ks * 32 + (lane >> 4) * 8);
      }
#pragma unroll
      for (int ct = 0; ct < 8; ++ct) {
        bv[ct] = *reinterpret_cast<const uint2*>(b1 + hh * 128 + ct * 16 + (lane >> 4) * 4);
        if (!half) bv[ct] = make_uint2(0u, 0u);
      }
    }
    __syncthreads();
    if (active) {
      unsigned char* ob = smem + K1_O + wave * (16 * O_STRIDE);
      const unsigned lbase = (unsigned)(size_t)smem + K1_W + ((lane >> 4) * 8 + ((lane & 15) >> 2)) * W_STRIDE + (lane & 3) * 8;
      uint2 o1[8];
#pragma unroll
      for (int ct = 0; ct < 8; ++ct) {
        f32x4 acc0 = {__uint_as_float(bv[ct].x << 16), __uint_as_float(bv[ct].x & 0xffff0000u),
                      __uint_as_float(bv[ct].y << 16), __uint_as_float(bv[ct].y & 0xffff0000u)};
        f32x4 acc1 = acc0;
#pragma unroll
        for (int ks = 0; ks < 4; ++ks) {
          const unsigned a0 = lbase + (ks * 32) * W_STRIDE + ct * 32;
          const s16x4 lo = __builtin_amdgcn_ds_read_tr16_b64_v4i16((lds_s16x4_ptr)(size_t)a0);
          const s16x4 hi = __builtin_amdgcn_ds_read_tr16_b64_v4i16((lds_s16x4_ptr)(size_t)(a0 + 4 * W_STRIDE));
          const bf16x8 a = {lo[0], lo[1], lo[2], lo[3], hi[0], hi[1], hi[2], hi[3]};
          acc0 = __builtin_amdgcn_mfma_f32_16x16x32_bf16(a, xb0[ks], acc0, 0, 0, 0);
          acc1 = __builtin_amdgcn_mfma_f32_16x16x32_bf16(a, xb1[ks], acc1, 0, 0, 0);
        }
        uint2 o;
        o.x = pack2(acc0[0] * NEG_LOG2E, acc0[1] * NEG_LOG2E);
        o.y = pack2(acc0[2] * NEG_LOG2E, acc0[3] * NEG_LOG2E);
        *reinterpret_cast<uint2*>(ob + (lane & 15) * O_STRIDE + ct * 32 + (lane >> 4) * 8) = o;
        o1[ct].x = pack2(acc1[0] * NEG_LOG2E, acc1[1] * NEG_LOG2E);
        o1[ct].y = pack2(acc1[2] * NEG_LOG2E, acc1[3] * NEG_LOG2E);
      }
      unsigned short* dstp = (half ? Qp : Pp) + (size_t)(nt0 * 16) * HH + hh * 128;
#pragma unroll
      for (int r = 0; r < 4; ++r) {
        const int row = r * 4 + (lane >> 4);
        const uint4 v = *reinterpret_cast<const uint4*>(ob + row * O_STRIDE + (lane & 15) * 16);
        *reinterpret_cast<uint4*>(dstp + (size_t)row * HH + (lane & 15) * 8) = v;
      }
      if (nt0 + 1 < NN / 16) {
        __builtin_amdgcn_wave_barrier();
#pragma unroll
        for (int ct = 0; ct < 8; ++ct)
          *reinterpret_cast<uint2*>(ob + (lane & 15) * O_STRIDE + ct * 32 + (lane >> 4) * 8) = o1[ct];
#pragma unroll
        for (int r = 0; r < 4; ++r) {
          const int row = r * 4 + (lane >> 4);
          const uint4 v = *reinterpret_cast<const uint4*>(ob + row * O_STRIDE + (lane & 15) * 16);
          *reinterpret_cast<uint4*>(dstp + (size_t)(16 + row) * HH + (lane & 15) * 8) = v;
        }
      }
    }
  }
}

constexpr int QS_STRIDE = 1040;
constexpr int LCAP = 2048;
constexpr int K2_QS   = 0;
constexpr int K2_Q    = K2_QS + NPB * QS_STRIDE;
constexpr int K2_CAR  = K2_Q + NPB * HH * 2;
constexpr int K2_KEY  = K2_CAR + 16 * HH * 4;
constexpr int K2_CM   = K2_KEY + G * CAP * 4;
constexpr int K2_L    = K2_CM + NPB * G * 4;
constexpr int K2_DEG  = K2_L + (LCAP + 32) * 4;
constexpr int K2_RP   = K2_DEG + 64 * 4;
constexpr int K2_CN   = K2_RP + 64 * 4;
constexpr int K2_BC   = K2_CN + 16 * 4;
constexpr int K2_FLAG = K2_BC + 64 * 4;
constexpr int K2_LDS  = K2_FLAG + 16;

__device__ __forceinline__ float sig_t(float t) {
  return __builtin_amdgcn_rcpf(1.0f + __builtin_amdgcn_exp2f(t));
}

struct Acc { float a0, a1, a2, a3; };

__device__ __forceinline__ void edge_acc(Acc& a, unsigned rec, uint2 v, const float4& q,
                                         float wq0, float wq1, float wq2, float wq3) {
  const float e = __uint_as_float(rec & 0xffff0000u);
  const float t0 = fmaf(e, wq0, q.x) + __uint_as_float(v.x << 16);
  const float t1 = fmaf(e, wq1, q.y) + __uint_as_float(v.x & 0xffff0000u);
  const float t2 = fmaf(e, wq2, q.z) + __uint_as_float(v.y << 16);
  const float t3 = fmaf(e, wq3, q.w) + __uint_as_float(v.y & 0xffff0000u);
  a.a0 = fmaf(t0, sig_t(t0), a.a0);
  a.a1 = fmaf(t1, sig_t(t1), a.a1);
  a.a2 = fmaf(t2, sig_t(t2), a.a2);
  a.a3 = fmaf(t3, sig_t(t3), a.a3);
}

__device__ __forceinline__ float4 load_q(const unsigned char* qrow, int lane) {
  const uint2 v = *reinterpret_cast<const uint2*>(qrow + lane * 8);
  return make_float4(__uint_as_float(v.x << 16), __uint_as_float(v.x & 0xffff0000u),
                     __uint_as_float(v.y << 16), __uint_as_float(v.y & 0xffff0000u));
}

__device__ __forceinline__ int wave_incl_scan(int v, int lane) {
#pragma unroll
  for (int o = 1; o < 64; o <<= 1) {
    const int t = __shfl_up(v, o, 64);
    if (lane >= o) v += t;
  }
  return v;
}

struct Grp { unsigned r0, r1, r2, r3; uint2 v0, v1, v2, v3; };

__device__ __forceinline__ uint2 gather_row(__amdgpu_buffer_rsrc_t rsrc, unsigned loff, unsigned rec) {
  typedef unsigned u32x2v __attribute__((ext_vector_type(2)));
  const u32x2v v = __builtin_amdgcn_raw_buffer_load_b64(rsrc, (int)loff, (int)((rec & 0xffffu) * (HH * 2)), 0);
  return make_uint2(v[0], v[1]);
}

__global__ __launch_bounds__(1024, 8) void k2_agg(
    const unsigned short* __restrict__ Pp, const unsigned short* __restrict__ Qp,
    const uint2* __restrict__ bk, const unsigned* __restrict__ cntg,
    const unsigned short* __restrict__ w2T, const unsigned short* __restrict__ w1,
    const unsigned short* __restrict__ b2,
    const unsigned short* __restrict__ eattr, const int* __restrict__ src, const int* __restrict__ dst,
    unsigned short* __restrict__ out) {
  __shared__ __attribute__((aligned(16))) unsigned char smem[K2_LDS];
  unsigned char* QS = smem + K2_QS;
  unsigned char* Qs = smem + K2_Q;
  float* car = reinterpret_cast<float*>(smem + K2_CAR);
  unsigned* keys = reinterpret_cast<unsigned*>(smem + K2_KEY);
  unsigned* cm = reinterpret_cast<unsigned*>(smem + K2_CM);
  unsigned* L = reinterpret_cast<unsigned*>(smem + K2_L);
  int* degs = reinterpret_cast<int*>(smem + K2_DEG);
  int* rps = reinterpret_cast<int*>(smem + K2_RP);
  int* cnode = reinterpret_cast<int*>(smem + K2_CN);
  unsigned* bcnt = reinterpret_cast<unsigned*>(smem + K2_BC);
  int* flag = reinterpret_cast<int*>(smem + K2_FLAG);

  const int tid = threadIdx.x;
  const int lane = tid & 63;
  const int wave = __builtin_amdgcn_readfirstlane(tid >> 6);
  const int n0 = blockIdx.x * NPB;

  const int mych = tid >> 4;
  const int mysl = tid & 15;
  const unsigned mycnt = cntg[blockIdx.x * G + mych];
  uint2 rr4[SPT];
  {
    const uint2* bp = bk + ((size_t)blockIdx.x * G + mych) * CAP + mysl;
#pragma unroll
    for (int j = 0; j < SPT; ++j) rr4[j] = bp[16 * j];
  }
  {
    const uint4* qg = reinterpret_cast<const uint4*>(Qp + (size_t)n0 * HH);
    for (int i = tid; i < NPB * HH / 8; i += 1024) reinterpret_cast<uint4*>(Qs)[i] = qg[i];
  }
  float wq0, wq1, wq2, wq3;
  {
    const uint2 wv = *reinterpret_cast<const uint2*>(w1 + (size_t)(2 * DD) * HH + lane * 4);
    wq0 = __uint_as_float(wv.x << 16) * NEG_LOG2E;
    wq1 = __uint_as_float(wv.x & 0xffff0000u) * NEG_LOG2E;
    wq2 = __uint_as_float(wv.y << 16) * NEG_LOG2E;
    wq3 = __uint_as_float(wv.y & 0xffff0000u) * NEG_LOG2E;
  }
  for (int i = tid; i < NPB * G; i += 1024) cm[i] = 0u;
  if (tid == 0) *flag = 0;
  if (tid < 16) cnode[tid] = -1;
  __syncthreads();

  unsigned rk[SPT], rv[SPT];
#pragma unroll
  for (int j = 0; j < SPT; ++j) {
    const bool ok = (unsigned)(mysl + 16 * j) < mycnt;
    rk[j] = ok ? rr4[j].x : 0xffffffffu;
    rv[j] = rr4[j].y;
  }
  if (mycnt > (unsigned)CAP) *flag = 1;
#pragma unroll
  for (int j = 0; j < SPT; ++j) {
    keys[mych * CAP + mysl + 16 * j] = rk[j];
    if (rk[j] != 0xffffffffu && mycnt <= (unsigned)CAP) atomicAdd(&cm[(rk[j] >> 16) * G + mych], 1u);
  }
  __syncthreads();

#pragma unroll
  for (int s = 0; s < NPW; ++s) {
    const int nl = wave + 16 * s;
    if (nl < NPB) {
      const int c = (int)cm[nl * G + lane];
      const int incl = wave_incl_scan(c, lane);
      cm[nl * G + lane] = (unsigned)(incl - c);
      degs[nl] = __builtin_amdgcn_readlane(incl, 63);
    }
  }
  __syncthreads();

  const int mydeg = lane < NPB ? degs[lane] : 0;
  const int pincl = wave_incl_scan(mydeg, lane);
  const int rp = pincl - mydeg;
  const int T = __builtin_amdgcn_readlane(pincl, 63);
  const bool fast = (T <= LCAP) && (__builtin_amdgcn_readfirstlane(*flag) == 0);

  if (fast) {
    if (wave == 1) rps[lane] = rp;
    {
      unsigned mx = mycnt;
#pragma unroll
      for (int o = 16; o < 64; o <<= 1) mx = max(mx, (unsigned)__shfl_xor((int)mx, o, 64));
      const int nrow = __builtin_amdgcn_readfirstlane((int)((mx + 15u) >> 4));
      unsigned rank[SPT];
#pragma unroll
      for (int j = 0; j < SPT; ++j) rank[j] = 0u;
      const int nq = __builtin_amdgcn_readfirstlane((int)((mx + 3u) >> 2));
      for (int s4 = 0; s4 < nq; ++s4) {
        const uint4 kq = *reinterpret_cast<const uint4*>(keys + mych * CAP + s4 * 4);
#pragma unroll
        for (int j = 0; j < SPT; ++j) {
          if (j < nrow) {
            const unsigned kb = rk[j] & 0xffff0000u, kl = rk[j] & 0xffffu;
            rank[j] += (unsigned)((kq.x - kb) < kl) + (unsigned)((kq.y - kb) < kl) +
                       (unsigned)((kq.z - kb) < kl) + (unsigned)((kq.w - kb) < kl);
          }
        }
      }
#pragma unroll
      for (int j = 0; j < SPT; ++j) {
        if (j < nrow) {
          const unsigned nib = min(rk[j] >> 16, (unsigned)(NPB - 1));
          const int base = __shfl(rp, (int)nib, 64);
          if (rk[j] != 0xffffffffu) L[base + cm[nib * G + mych] + rank[j]] = rv[j];
        }
      }
    }
#pragma unroll
    for (int s = 0; s < NPW; ++s) {
      const int nl = wave + 16 * s;
      if (nl < NPB) {
        const int dg = __builtin_amdgcn_readlane(mydeg, nl);
        if (dg == 0) *reinterpret_cast<float4*>(QS + nl * QS_STRIDE + lane * 16) = make_float4(0.f, 0.f, 0.f, 0.f);
      }
    }
    if (wave == 0 && lane < 20) L[T + lane] = NULL_REC;
    __syncthreads();

    const int TG = (T + 3) >> 2;
    const int G0 = (wave * TG) >> 4;
    const int G1 = ((wave + 1) * TG) >> 4;
    if (G0 < G1) {
      int ni = __builtin_popcountll(__ballot(lane < NPB && rp <= G0 * 4)) - 1;
      bool mid = __builtin_amdgcn_readlane(rp, ni) < G0 * 4;
      int ende = ni + 1 < NPB ? __builtin_amdgcn_readlane(rp, ni + 1) : 0x7fffffff;
      if (ende >= T) ende = 0x7fffffff;
      float4 q = load_q(Qs + ni * (HH * 2), lane);
      Acc acc = {0.f, 0.f, 0.f, 0.f};
      const unsigned loff = (unsigned)lane * 8u;
      const __amdgpu_buffer_rsrc_t prsrc = __builtin_amdgcn_make_buffer_rsrc(
          const_cast<unsigned short*>(Pp), (short)0, (int)((NN + 1) * HH * 2), 0x00020000);
      unsigned rcur = L[G0 * 4 + lane];
      unsigned rnxt = L[min(G0 * 4 + 64 + lane, LCAP + 31)];

      auto flush = [&]() {
        float* dstrow = mid ? (car + wave * HH) : reinterpret_cast<float*>(QS + ni * QS_STRIDE);
        *reinterpret_cast<float4*>(dstrow + lane * 4) = make_float4(acc.a0, acc.a1, acc.a2, acc.a3);
        if (mid) cnode[wave] = ni;
        mid = false;
        acc.a0 = 0.f; acc.a1 = 0.f; acc.a2 = 0.f; acc.a3 = 0.f;
      };
      auto issue = [&](Grp& b, int g) {
        const int rel = (g - G0) * 4;
        if ((rel & 63) == 0 && rel > 0) {
          rcur = rnxt;
          rnxt = L[min(G0 * 4 + rel + 64 + lane, LCAP + 31)];
        }
        b.r0 = __builtin_amdgcn_readlane(rcur, (rel & 63) + 0);
        b.r1 = __builtin_amdgcn_readlane(rcur, (rel & 63) + 1);
        b.r2 = __builtin_amdgcn_readlane(rcur, (rel & 63) + 2);
        b.r3 = __builtin_amdgcn_readlane(rcur, (rel & 63) + 3);
        b.v0 = gather_row(prsrc, loff, b.r0);
        b.v1 = gather_row(prsrc, loff, b.r1);
        b.v2 = gather_row(prsrc, loff, b.r2);
        b.v3 = gather_row(prsrc, loff, b.r3);
      };
      auto boundary = [&](int e) {
        if (e == ende) {
          flush();
          do {
            ++ni;
            ende = __builtin_amdgcn_readfirstlane(ni + 1 < NPB ? rps[ni + 1] : T);
          } while (e == ende);
          if (ende >= T) ende = 0x7fffffff;
          q = load_q(Qs + ni * (HH * 2), lane);
        }
      };
      auto compute = [&](const Grp& b, int g) {
        boundary(g * 4 + 0); edge_acc(acc, b.r0, b.v0, q, wq0, wq1, wq2, wq3);
        boundary(g * 4 + 1); edge_acc(acc, b.r1, b.v1, q, wq0, wq1, wq2, wq3);
        boundary(g * 4 + 2); edge_acc(acc, b.r2, b.v2, q, wq0, wq1, wq2, wq3);
        boundary(g * 4 + 3); edge_acc(acc, b.r3, b.v3, q, wq0, wq1, wq2, wq3);
      };

      Grp A, B, C;
      issue(A, G0);
      __builtin_amdgcn_sched_barrier(0);
      issue(B, G0 + 1);
      __builtin_amdgcn_sched_barrier(0);
      for (int g = G0; g < G1; g += 3) {
        issue(C, g + 2); compute(A, g);
        issue(A, g + 3); if (g + 1 < G1) compute(B, g + 1);
        issue(B, g + 4); if (g + 2 < G1) compute(C, g + 2);
      }
      flush();
    }
  } else {
#pragma unroll 1
    for (int s = 0; s < NPW; ++s) {
      const int nl = wave + 16 * s;
      if (nl < NPB) {
        const float4 q = load_q(Qs + nl * (HH * 2), lane);
        Acc acc = {0.f, 0.f, 0.f, 0.f};
        int total = 0;
        for (int e0 = 0; e0 < EE; e0 += 64) {
          const bool hit = dst[e0 + lane] == n0 + nl;
          unsigned long long m = __ballot(hit);
          if (m != 0ull) {
            const unsigned rec = hit ? (((unsigned)src[e0 + lane] & 0xffffu) | ((unsigned)eattr[e0 + lane] << 16)) : NULL_REC;
            total += __builtin_popcountll(m);
            while (m != 0ull) {
              const int i = __builtin_ctzll(m);
              m &= m - 1ull;
              const unsigned rr = __builtin_amdgcn_readlane(rec, i);
              const uint2 v = *reinterpret_cast<const uint2*>(Pp + (size_t)(rr & 0xffffu) * HH + lane * 4);
              edge_acc(acc, rr, v, q, wq0, wq1, wq2, wq3);
            }
          }
        }
        *reinterpret_cast<float4*>(QS + nl * QS_STRIDE + lane * 16) = make_float4(acc.a0, acc.a1, acc.a2, acc.a3);
        degs[nl] = total;
      }
    }
  }

  const int oct = wave & 7;
  bf16x8 wf[8];
#pragma unroll
  for (int ks = 0; ks < 8; ++ks)
    wf[ks] = *reinterpret_cast<const bf16x8*>(w2T + (oct * 16 + (lane & 15)) * HH + ks * 32 + (lane >> 4) * 8);
  __syncthreads();

#pragma unroll
  for (int s = 0; s < NPW; ++s) {
    const int nl = wave + 16 * s;
    if (nl < NPB) {
      unsigned char* row = QS + nl * QS_STRIDE;
      float4 v = *reinterpret_cast<const float4*>(row + lane * 16);
      unsigned long long cm_ = __ballot(lane < 16 && cnode[lane & 15] == nl);
      while (cm_ != 0ull) {
        const int w = __builtin_ctzll(cm_);
        cm_ &= cm_ - 1ull;
        const float4 c = *reinterpret_cast<const float4*>(car + w * HH + lane * 4);
        v.x += c.x; v.y += c.y; v.z += c.z; v.w += c.w;
      }
      const float s0 = v.x * NEG_LN2, s1 = v.y * NEG_LN2, s2 = v.z * NEG_LN2, s3 = v.w * NEG_LN2;
      const unsigned short h0 = f2bf(s0), h1 = f2bf(s1), h2 = f2bf(s2), h3 = f2bf(s3);
      uint2 hv, lv;
      hv.x = (unsigned)h0 | ((unsigned)h1 << 16);
      hv.y = (unsigned)h2 | ((unsigned)h3 << 16);
      lv.x = pack2(s0 - bf2f(h0), s1 - bf2f(h1));
      lv.y = pack2(s2 - bf2f(h2), s3 - bf2f(h3));
      __builtin_amdgcn_wave_barrier();
      *reinterpret_cast<uint2*>(row + lane * 8) = hv;
      *reinterpret_cast<uint2*>(row + 512 + lane * 8) = lv;
    }
  }
  __syncthreads();

  for (int ntile = wave >> 3; ntile < (NPB + 15) / 16; ntile += 2) {
    f32x4 acc = {0.f, 0.f, 0.f, 0.f};
    const int srow = ntile * 16 + (lane & 15);
#pragma unroll
    for (int ks = 0; ks < 8; ++ks) {
      const bf16x8 bh = *reinterpret_cast<const bf16x8*>(QS + srow * QS_STRIDE + ks * 64 + (lane >> 4) * 16);
      const bf16x8 bl = *reinterpret_cast<const bf16x8*>(QS + srow * QS_STRIDE + 512 + ks * 64 + (lane >> 4) * 16);
      acc = __builtin_amdgcn_mfma_f32_16x16x32_bf16(wf[ks], bh, acc, 0, 0, 0);
      acc = __builtin_amdgcn_mfma_f32_16x16x32_bf16(wf[ks], bl, acc, 0, 0, 0);
    }
    if (srow < NPB) {
      const int oc = oct * 16 + (lane >> 4) * 4;
      const float dg = (float)degs[srow];
      uint2 o;
      o.x = pack2(acc[0] + dg * bf2f(b2[oc + 0]), acc[1] + dg * bf2f(b2[oc + 1]));
      o.y = pack2(acc[2] + dg * bf2f(b2[oc + 2]), acc[3] + dg * bf2f(b2[oc + 3]));
      *reinterpret_cast<uint2*>(out + (size_t)(n0 + srow) * DD + oc) = o;
    }
  }
}

}

extern "C" void kernel_launch(void* const* d_in, const int* in_sizes, int n_in,
                              void* d_out, int out_size, void* d_ws, size_t ws_size,
                              hipStream_t stream) {
  const unsigned short* x  = (const unsigned short*)d_in[0];
  const unsigned short* ea = (const unsigned short*)d_in[1];
  const int* src = (const int*)d_in[2];
  const int* dst = (const int*)d_in[3];
  const unsigned short* w1 = (const unsigned short*)d_in[4];
  const unsigned short* b1 = (const unsigned short*)d_in[5];
  const unsigned short* w2 = (const unsigned short*)d_in[6];
  const unsigned short* b2 = (const unsigned short*)d_in[7];
  unsigned char* ws = (unsigned char*)d_ws;
  unsigned short* Pp = (unsigned short*)(ws + WS_P);
  unsigned short* Qp = (unsigned short*)(ws + WS_Q);
  uint2* bk = (uint2*)(ws + WS_BK);
  unsigned* cntg = (unsigned*)(ws + WS_CNT);
  unsigned short* w2T = (unsigned short*)(ws + WS_W2T);

  k1_prep<<<K1_BLOCKS, 1024, 0, stream>>>(x, ea, src, dst, w1, b1, w2, Pp, Qp, bk, cntg, w2T);
  k2_agg<<<NB, 1024, 0, stream>>>(Pp, Qp, bk, cntg, w2T, w1, b2, ea, src, dst, (unsigned short*)d_out);
}
```

```cpp
#include <hip/hip_runtime.h>
#include <hip/hip_bf16.h>
#include <stdint.h>

using bf16x8 = __attribute__((ext_vector_type(8))) short;
using s16x4  = __attribute__((ext_vector_type(4))) short;
using f32x4  = __attribute__((ext_vector_type(4))) float;
typedef __attribute__((address_space(3))) s16x4* lds_s16x4_ptr;

namespace {

constexpr int NN  = 10000;
constexpr int EE  = 320000;
constexpr int DD  = 128;
constexpr int HH  = 256;
constexpr int G   = 64;
constexpr int CH  = EE / G;
constexpr int NPB = 20;
constexpr int NB  = NN / NPB;
constexpr int CAP = 32;
constexpr int SPT = CAP / 16;
constexpr int NPW = (NPB + 15) / 16;
constexpr int GEMM_BLOCKS = 160;
constexpr int K1_BLOCKS = G + GEMM_BLOCKS;
constexpr int W_STRIDE = 288;
constexpr int O_STRIDE = 272;

constexpr size_t WS_P    = 0;
constexpr size_t WS_Q    = WS_P + (size_t)(NN + 1) * HH * 2;
constexpr size_t WS_BK   = WS_Q + (size_t)NN * HH * 2;
constexpr size_t WS_CNT  = WS_BK + (size_t)NB * G * CAP * 8;
constexpr size_t WS_W2T  = WS_CNT + (size_t)NB * G * 4;
constexpr size_t WS_END  = WS_W2T + (size_t)DD * HH * 2;

constexpr float NEG_LOG2E = -1.4426950408889634f;
constexpr float NEG_LN2   = -0.6931471805599453f;
constexpr unsigned NULL_REC = (unsigned)NN;
constexpr unsigned short NULL_VAL = 0x7149;

__device__ __forceinline__ float bf2f(unsigned short u) { return __uint_as_float(((unsigned)u) << 16); }
__device__ __forceinline__ unsigned short f2bf(float f) {
  __hip_bfloat16 h = __float2bfloat16(f);
  return *reinterpret_cast<unsigned short*>(&h);
}
__device__ __forceinline__ unsigned pack2(float a, float b) {
  return (unsigned)f2bf(a) | ((unsigned)f2bf(b) << 16);
}

constexpr int K1_W = 0;
constexpr int K1_O = DD * W_STRIDE;
constexpr int K1_LDS = K1_O + 8 * 16 * O_STRIDE;

__global__ __launch_bounds__(1024) void k1_prep(
    const unsigned short* __restrict__ x, const unsigned short* __restrict__ eattr,
    const int* __restrict__ src, const int* __restrict__ dst,
    const unsigned short* __restrict__ w1, const unsigned short* __restrict__ b1,
    const unsigned short* __restrict__ w2,
    unsigned short* __restrict__ Pp, unsigned short* __restrict__ Qp,
    uint2* __restrict__ bk, unsigned* __restrict__ cntg,
    unsigned short* __restrict__ w2T) {
  __shared__ __attribute__((aligned(16))) unsigned char smem[K1_LDS];
  const int tid = threadIdx.x;
  const int lane = tid & 63;
  const int wave = tid >> 6;
  const int bid = blockIdx.x;

  if (bid < G) {
    unsigned* hist = reinterpret_cast<unsigned*>(smem);
    const int ebase = bid * CH;
    int d[5];
    unsigned rec[5];
#pragma unroll
    for (int i = 0; i < 5; ++i) {
      const int idx = i * 1024 + tid;
      d[i] = 0; rec[i] = 0;
      if (idx < CH) {
        d[i] = dst[ebase + idx];
        rec[i] = ((unsigned)src[ebase + idx] & 0xffffu) | ((unsigned)eattr[ebase + idx] << 16);
      }
    }
    if (tid < 512) hist[tid] = 0u;
    __syncthreads();
#pragma unroll
    for (int i = 0; i < 5; ++i) {
      const int idx = i * 1024 + tid;
      if (idx < CH) {
        const int bin = d[i] / NPB;
        const int nib = d[i] - bin * NPB;
        const unsigned slot = atomicAdd(&hist[bin], 1u);
        if (slot < CAP) bk[((size_t)bin * G + bid) * CAP + slot] = make_uint2(((unsigned)nib << 16) | (unsigned)idx, rec[i]);
      }
    }
    __syncthreads();
    if (tid < NB) cntg[tid * G + bid] = hist[tid];
  } else {
    const int gb = bid - G;
    const int half = gb / 80;
    const int hh = (gb / 40) & 1;
    const int tg = gb % 40;
#pragma unroll
    for (int i = 0; i < 2; ++i) {
      const int k = (tid >> 4) + 64 * i;
      const int c8 = (tid & 15) * 8;
      const uint4 v = *reinterpret_cast<const uint4*>(w1 + (size_t)(half * DD + k) * HH + hh * 128 + c8);
      *reinterpret_cast<uint4*>(smem + K1_W + k * W_STRIDE + c8 * 2) = v;
    }
    if (gb < 64 && tid >= 512) {
      const int i = gb * 512 + tid - 512;
      const int k = i >> 7, oc = i & 127;
      w2T[oc * HH + k] = w2[i];
    }
    if (gb == 64 && tid >= 512 && tid < 512 + HH) Pp[(size_t)NN * HH + tid - 512] = NULL_VAL;
    const int nt0 = tg * 16 + wave * 2;
    const bool active = wave < 8 && nt0 < NN / 16;
    const int ntc0 = nt0 < NN / 16 ? nt0 : NN / 16 - 1;
    const int ntc1 = nt0 + 1 < NN / 16 ? nt0 + 1 : NN / 16 - 1;
    bf16x8 xb0[4], xb1[4];
    uint2 bv[8];
    if (active) {
#pragma unroll
      for (int ks = 0; ks < 4; ++ks) {
        xb0[ks] = *reinterpret_cast<const bf16x8*>(x + (size_t)(ntc0 * 16 + (lane & 15)) * DD + ks * 32 + (lane >> 4) * 8);
        xb1[ks] = *reinterpret_cast<const bf16x8*>(x + (size_t)(ntc1 * 16 + (lane & 15)) * DD + ks * 32 + (lane >> 4) * 8);
      }
#pragma unroll
      for (int ct = 0; ct < 8; ++ct) {
        bv[ct] = *reinterpret_cast<const uint2*>(b1 + hh * 128 + ct * 16 + (lane >> 4) * 4);
        if (!half) bv[ct] = make_uint2(0u, 0u);
      }
    }
    __syncthreads();
    if (active) {
      unsigned char* ob = smem + K1_O + wave * (16 * O_STRIDE);
      const unsigned lbase = (unsigned)(size_t)smem + K1_W + ((lane >> 4) * 8 + ((lane & 15) >> 2)) * W_STRIDE + (lane & 3) * 8;
      uint2 o1[8];
#pragma unroll
      for (int ct = 0; ct < 8; ++ct) {
        f32x4 acc0 = {__uint_as_float(bv[ct].x << 16), __uint_as_float(bv[ct].x & 0xffff0000u),
                      __uint_as_float(bv[ct].y << 16), __uint_as_float(bv[ct].y & 0xffff0000u)};
        f32x4 acc1 = acc0;
#pragma unroll
        for (int ks = 0; ks < 4; ++ks) {
          const unsigned a0 = lbase + (ks * 32) * W_STRIDE + ct * 32;
          const s16x4 lo = __builtin_amdgcn_ds_read_tr16_b64_v4i16((lds_s16x4_ptr)(size_t)a0);
          const s16x4 hi = __builtin_amdgcn_ds_read_tr16_b64_v4i16((lds_s16x4_ptr)(size_t)(a0 + 4 * W_STRIDE));
          const bf16x8 a = {lo[0], lo[1], lo[2], lo[3], hi[0], hi[1], hi[2], hi[3]};
          acc0 = __builtin_amdgcn_mfma_f32_16x16x32_bf16(a, xb0[ks], acc0, 0, 0, 0);
          acc1 = __builtin_amdgcn_mfma_f32_16x16x32_bf16(a, xb1[ks], acc1, 0, 0, 0);
        }
        uint2 o;
        o.x = pack2(acc0[0] * NEG_LOG2E, acc0[1] * NEG_LOG2E);
        o.y = pack2(acc0[2] * NEG_LOG2E, acc0[3] * NEG_LOG2E);
        *reinterpret_cast<uint2*>(ob + (lane & 15) * O_STRIDE + ct * 32 + (lane >> 4) * 8) = o;
        o1[ct].x = pack2(acc1[0] * NEG_LOG2E, acc1[1] * NEG_LOG2E);
        o1[ct].y = pack2(acc1[2] * NEG_LOG2E, acc1[3] * NEG_LOG2E);
      }
      const __amdgpu_buffer_rsrc_t wrs = __builtin_amdgcn_make_buffer_rsrc(half ? Qp : Pp, (short)0, (int)(NN * HH * 2), 0x00020000);
#pragma unroll
      for (int r = 0; r < 4; ++r) {
        const int row = r * 4 + (lane >> 4);
        const uint4 v = *reinterpret_cast<const uint4*>(ob + row * O_STRIDE + (lane & 15) * 16);
        { typedef unsigned u32x4v __attribute__((ext_vector_type(4))); const u32x4v vv = {v.x, v.y, v.z, v.w};
          __builtin_amdgcn_raw_buffer_store_b128(vv, wrs, (int)(((size_t)(nt0 * 16 + row) * HH + hh * 128 + (lane & 15) * 8) * 2), 0, 16); }
      }
      if (nt0 + 1 < NN / 16) {
        __builtin_amdgcn_wave_barrier();
#pragma unroll
        for (int ct = 0; ct < 8; ++ct)
          *reinterpret_cast<uint2*>(ob + (lane & 15) * O_STRIDE + ct * 32 + (lane >> 4) * 8) = o1[ct];
#pragma unroll
        for (int r = 0; r < 4; ++r) {
          const int row = r * 4 + (lane >> 4);
          const uint4 v = *reinterpret_cast<const uint4*>(ob + row * O_STRIDE + (lane & 15) * 16);
          { typedef unsigned u32x4v __attribute__((ext_vector_type(4))); const u32x4v vv = {v.x, v.y, v.z, v.w};
            __builtin_amdgcn_raw_buffer_store_b128(vv, wrs, (int)(((size_t)(nt0 * 16 + 16 + row) * HH + hh * 128 + (lane & 15) * 8) * 2), 0, 16); }
        }
      }
    }
  }
}

constexpr int QS_STRIDE = 1040;
constexpr int LCAP = 2048;
constexpr int K2_QS   = 0;
constexpr int K2_Q    = K2_QS + NPB * QS_STRIDE;
constexpr int K2_CAR  = K2_Q + NPB * HH * 2;
constexpr int K2_KEY  = K2_CAR + 16 * HH * 4;
constexpr int K2_CM   = K2_KEY + G * CAP * 4;
constexpr int K2_L    = K2_CM + NPB * G * 4;
constexpr int K2_DEG  = K2_L + (LCAP + 32) * 4;
constexpr int K2_RP   = K2_DEG + 64 * 4;
constexpr int K2_CN   = K2_RP + 64 * 4;
constexpr int K2_BC   = K2_CN + 16 * 4;
constexpr int K2_FLAG = K2_BC + 64 * 4;
constexpr int K2_LDS  = K2_FLAG + 16;

__device__ __forceinline__ float sig_t(float t) {
  return __builtin_amdgcn_rcpf(1.0f + __builtin_amdgcn_exp2f(t));
}

struct Acc { float a0, a1, a2, a3; };

__device__ __forceinline__ void edge_acc(Acc& a, unsigned rec, uint2 v, const float4& q,
                                         float wq0, float wq1, float wq2, float wq3) {
  const float e = __uint_as_float(rec & 0xffff0000u);
  const float t0 = fmaf(e, wq0, q.x) + __uint_as_float(v.x << 16);
  const float t1 = fmaf(e, wq1, q.y) + __uint_as_float(v.x & 0xffff0000u);
  const float t2 = fmaf(e, wq2, q.z) + __uint_as_float(v.y << 16);
  const float t3 = fmaf(e, wq3, q.w) + __uint_as_float(v.y & 0xffff0000u);
  a.a0 = fmaf(t0, sig_t(t0), a.a0);
  a.a1 = fmaf(t1, sig_t(t1), a.a1);
  a.a2 = fmaf(t2, sig_t(t2), a.a2);
  a.a3 = fmaf(t3, sig_t(t3), a.a3);
}

__device__ __forceinline__ float4 load_q(const unsigned char* qrow, int lane) {
  const uint2 v = *reinterpret_cast<const uint2*>(qrow + lane * 8);
  return make_float4(__uint_as_float(v.x << 16), __uint_as_float(v.x & 0xffff0000u),
                     __uint_as_float(v.y << 16), __uint_as_float(v.y & 0xffff0000u));
}

__device__ __forceinline__ int wave_incl_scan(int v, int lane) {
#pragma unroll
  for (int o = 1; o < 64; o <<= 1) {
    const int t = __shfl_up(v, o, 64);
    if (lane >= o) v += t;
  }
  return v;
}

struct Grp { unsigned r0, r1, r2, r3; uint2 v0, v1, v2, v3; };

__device__ __forceinline__ uint2 gather_row(__amdgpu_buffer_rsrc_t rsrc, unsigned loff, unsigned rec) {
  typedef unsigned u32x2v __attribute__((ext_vector_type(2)));
  const u32x2v v = __builtin_amdgcn_raw_buffer_load_b64(rsrc, (int)loff, (int)((rec & 0xffffu) * (HH * 2)), 0);
  return make_uint2(v[0], v[1]);
}

__global__ __launch_bounds__(1024, 8) void k2_agg(
    const unsigned short* __restrict__ Pp, const unsigned short* __restrict__ Qp,
    const uint2* __restrict__ bk, const unsigned* __restrict__ cntg,
    const unsigned short* __restrict__ w2T, const unsigned short* __restrict__ w1,
    const unsigned short* __restrict__ b2,
    const unsigned short* __restrict__ eattr, const int* __restrict__ src, const int* __restrict__ dst,
    unsigned short* __restrict__ out) {
  __shared__ __attribute__((aligned(16))) unsigned char smem[K2_LDS];
  unsigned char* QS = smem + K2_QS;
  unsigned char* Qs = smem + K2_Q;
  float* car = reinterpret_cast<float*>(smem + K2_CAR);
  unsigned* keys = reinterpret_cast<unsigned*>(smem + K2_KEY);
  unsigned* cm = reinterpret_cast<unsigned*>(smem + K2_CM);
  unsigned* L = reinterpret_cast<unsigned*>(smem + K2_L);
  int* degs = reinterpret_cast<int*>(smem + K2_DEG);
  int* rps = reinterpret_cast<int*>(smem + K2_RP);
  int* cnode = reinterpret_cast<int*>(smem + K2_CN);
  unsigned* bcnt = reinterpret_cast<unsigned*>(smem + K2_BC);
  int* flag = reinterpret_cast<int*>(smem + K2_FLAG);

  const int tid = threadIdx.x;
  const int lane = tid & 63;
  const int wave = __builtin_amdgcn_readfirstlane(tid >> 6);
  const int n0 = blockIdx.x * NPB;

  const int mych = tid >> 4;
  const int mysl = tid & 15;
  const unsigned mycnt = cntg[blockIdx.x * G + mych];
  uint2 rr4[SPT];
  {
    const uint2* bp = bk + ((size_t)blockIdx.x * G + mych) * CAP + mysl;
#pragma unroll
    for (int j = 0; j < SPT; ++j) rr4[j] = bp[16 * j];
  }
  {
    const uint4* qg = reinterpret_cast<const uint4*>(Qp + (size_t)n0 * HH);
    for (int i = tid; i < NPB * HH / 8; i += 1024) reinterpret_cast<uint4*>(Qs)[i] = qg[i];
  }
  float wq0, wq1, wq2, wq3;
  {
    const uint2 wv = *reinterpret_cast<const uint2*>(w1 + (size_t)(2 * DD) * HH + lane * 4);
    wq0 = __uint_as_float(wv.x << 16) * NEG_LOG2E;
    wq1 = __uint_as_float(wv.x & 0xffff0000u) * NEG_LOG2E;
    wq2 = __uint_as_float(wv.y << 16) * NEG_LOG2E;
    wq3 = __uint_as_float(wv.y & 0xffff0000u) * NEG_LOG2E;
  }
  for (int i = tid; i < NPB * G; i += 1024) cm[i] = 0u;
  if (tid == 0) *flag = 0;
  if (tid < 16) cnode[tid] = -1;
  __syncthreads();

  unsigned rk[SPT], rv[SPT];
#pragma unroll
  for (int j = 0; j < SPT; ++j) {
    const bool ok = (unsigned)(mysl + 16 * j) < mycnt;
    rk[j] = ok ? rr4[j].x : 0xffffffffu;
    rv[j] = rr4[j].y;
  }
  if (mycnt > (unsigned)CAP) *flag = 1;
#pragma unroll
  for (int j = 0; j < SPT; ++j) {
    keys[mych * CAP + mysl + 16 * j] = rk[j];
    if (rk[j] != 0xffffffffu && mycnt <= (unsigned)CAP) atomicAdd(&cm[(rk[j] >> 16) * G + mych], 1u);
  }
  __syncthreads();

#pragma unroll
  for (int s = 0; s < NPW; ++s) {
    const int nl = wave + 16 * s;
    if (nl < NPB) {
      const int c = (int)cm[nl * G + lane];
      const int incl = wave_incl_scan(c, lane);
      cm[nl * G + lane] = (unsigned)(incl - c);
      degs[nl] = __builtin_amdgcn_readlane(incl, 63);
    }
  }
  __syncthreads();

  const int mydeg = lane < NPB ? degs[lane] : 0;
  const int pincl = wave_incl_scan(mydeg, lane);
  const int rp = pincl - mydeg;
  const int T = __builtin_amdgcn_readlane(pincl, 63);
  const bool fast = (T <= LCAP) && (__builtin_amdgcn_readfirstlane(*flag) == 0);

  if (fast) {
    if (wave == 1) rps[lane] = rp;
    {
      unsigned mx = mycnt;
#pragma unroll
      for (int o = 16; o < 64; o <<= 1) mx = max(mx, (unsigned)__shfl_xor((int)mx, o, 64));
      const int nrow = __builtin_amdgcn_readfirstlane((int)((mx + 15u) >> 4));
      unsigned rank[SPT];
#pragma unroll
      for (int j = 0; j < SPT; ++j) rank[j] = 0u;
      const int nq = __builtin_amdgcn_readfirstlane((int)((mx + 3u) >> 2));
      for (int s4 = 0; s4 < nq; ++s4) {
        const uint4 kq = *reinterpret_cast<const uint4*>(keys + mych * CAP + s4 * 4);
#pragma unroll
        for (int j = 0; j < SPT; ++j) {
          if (j < nrow) {
            const unsigned kb = rk[j] & 0xffff0000u, kl = rk[j] & 0xffffu;
            rank[j] += (unsigned)((kq.x - kb) < kl) + (unsigned)((kq.y - kb) < kl) +
                       (unsigned)((kq.z - kb) < kl) + (unsigned)((kq.w - kb) < kl);
          }
        }
      }
#pragma unroll
      for (int j = 0; j < SPT; ++j) {
        if (j < nrow) {
          const unsigned nib = min(rk[j] >> 16, (unsigned)(NPB - 1));
          const int base = __shfl(rp, (int)nib, 64);
          if (rk[j] != 0xffffffffu) L[base + cm[nib * G + mych] + rank[j]] = rv[j];
        }
      }
    }
#pragma unroll
    for (int s = 0; s < NPW; ++s) {
      const int nl = wave + 16 * s;
      if (nl < NPB) {
        const int dg = __builtin_amdgcn_readlane(mydeg, nl);
        if (dg == 0) *reinterpret_cast<float4*>(QS + nl * QS_STRIDE + lane * 16) = make_float4(0.f, 0.f, 0.f, 0.f);
      }
    }
    if (wave == 0 && lane < 20) L[T + lane] = NULL_REC;
    __syncthreads();

    const int TG = (T + 3) >> 2;
    const int G0 = (wave * TG) >> 4;
    const int G1 = ((wave + 1) * TG) >> 4;
    if (G0 < G1) {
      int ni = __builtin_popcountll(__ballot(lane < NPB && rp <= G0 * 4)) - 1;
      bool mid = __builtin_amdgcn_readlane(rp, ni) < G0 * 4;
      int ende = ni + 1 < NPB ? __builtin_amdgcn_readlane(rp, ni + 1) : 0x7fffffff;
      if (ende >= T) ende = 0x7fffffff;
      float4 q = load_q(Qs + ni * (HH * 2), lane);
      Acc acc = {0.f, 0.f, 0.f, 0.f};
      const unsigned loff = (unsigned)lane * 8u;
      const __amdgpu_buffer_rsrc_t prsrc = __builtin_amdgcn_make_buffer_rsrc(
          const_cast<unsigned short*>(Pp), (short)0, (int)((NN + 1) * HH * 2), 0x00020000);
      unsigned rcur = L[G0 * 4 + lane];
      unsigned rnxt = L[min(G0 * 4 + 64 + lane, LCAP + 31)];

      auto flush = [&]() {
        float* dstrow = mid ? (car + wave * HH) : reinterpret_cast<float*>(QS + ni * QS_STRIDE);
        *reinterpret_cast<float4*>(dstrow + lane * 4) = make_float4(acc.a0, acc.a1, acc.a2, acc.a3);
        if (mid) cnode[wave] = ni;
        mid = false;
        acc.a0 = 0.f; acc.a1 = 0.f; acc.a2 = 0.f; acc.a3 = 0.f;
      };
      auto issue = [&](Grp& b, int g) {
        const int rel = (g - G0) * 4;
        if ((rel & 63) == 0 && rel > 0) {
          rcur = rnxt;
          rnxt = L[min(G0 * 4 + rel + 64 + lane, LCAP + 31)];
        }
        b.r0 = __builtin_amdgcn_readlane(rcur, (rel & 63) + 0);
        b.r1 = __builtin_amdgcn_readlane(rcur, (rel & 63) + 1);
        b.r2 = __builtin_amdgcn_readlane(rcur, (rel & 63) + 2);
        b.r3 = __builtin_amdgcn_readlane(rcur, (rel & 63) + 3);
        b.v0 = gather_row(prsrc, loff, b.r0);
        b.v1 = gather_row(prsrc, loff, b.r1);
        b.v2 = gather_row(prsrc, loff, b.r2);
        b.v3 = gather_row(prsrc, loff, b.r3);
      };
      auto boundary = [&](int e) {
        if (e == ende) {
          flush();
          do {
            ++ni;
            ende = __builtin_amdgcn_readfirstlane(ni + 1 < NPB ? rps[ni + 1] : T);
          } while (e == ende);
          if (ende >= T) ende = 0x7fffffff;
          q = load_q(Qs + ni * (HH * 2), lane);
        }
      };
      auto compute = [&](const Grp& b, int g) {
        boundary(g * 4 + 0); edge_acc(acc, b.r0, b.v0, q, wq0, wq1, wq2, wq3);
        boundary(g * 4 + 1); edge_acc(acc, b.r1, b.v1, q, wq0, wq1, wq2, wq3);
        boundary(g * 4 + 2); edge_acc(acc, b.r2, b.v2, q, wq0, wq1, wq2, wq3);
        boundary(g * 4 + 3); edge_acc(acc, b.r3, b.v3, q, wq0, wq1, wq2, wq3);
      };

      Grp A, B, C;
      issue(A, G0);
      __builtin_amdgcn_sched_barrier(0);
      issue(B, G0 + 1);
      __builtin_amdgcn_sched_barrier(0);
      for (int g = G0; g < G1; g += 3) {
        issue(C, g + 2); compute(A, g);
        issue(A, g + 3); if (g + 1 < G1) compute(B, g + 1);
        issue(B, g + 4); if (g + 2 < G1) compute(C, g + 2);
      }
      flush();
    }
  } else {
#pragma unroll 1
    for (int s = 0; s < NPW; ++s) {
      const int nl = wave + 16 * s;
      if (nl < NPB) {
        const float4 q = load_q(Qs + nl * (HH * 2), lane);
        Acc acc = {0.f, 0.f, 0.f, 0.f};
        int total = 0;
        for (int e0 = 0; e0 < EE; e0 += 64) {
          const bool hit = dst[e0 + lane] == n0 + nl;
          unsigned long long m = __ballot(hit);
          if (m != 0ull) {
            const unsigned rec = hit ? (((unsigned)src[e0 + lane] & 0xffffu) | ((unsigned)eattr[e0 + lane] << 16)) : NULL_REC;
            total += __builtin_popcountll(m);
            while (m != 0ull) {
              const int i = __builtin_ctzll(m);
              m &= m - 1ull;
              const unsigned rr = __builtin_amdgcn_readlane(rec, i);
              const uint2 v = *reinterpret_cast<const uint2*>(Pp + (size_t)(rr & 0xffffu) * HH + lane * 4);
              edge_acc(acc, rr, v, q, wq0, wq1, wq2, wq3);
            }
          }
        }
        *reinterpret_cast<float4*>(QS + nl * QS_STRIDE + lane * 16) = make_float4(acc.a0, acc.a1, acc.a2, acc.a3);
        degs[nl] = total;
      }
    }
  }

  const int oct = wave & 7;
  bf16x8 wf[8];
#pragma unroll
  for (int ks = 0; ks < 8; ++ks)
    wf[ks] = *reinterpret_cast<const bf16x8*>(w2T + (oct * 16 + (lane & 15)) * HH + ks * 32 + (lane >> 4) * 8);
  __syncthreads();

#pragma unroll
  for (int s = 0; s < NPW; ++s) {
    const int nl = wave + 16 * s;
    if (nl < NPB) {
      unsigned char* row = QS + nl * QS_STRIDE;
      float4 v = *reinterpret_cast<const float4*>(row + lane * 16);
      unsigned long long cm_ = __ballot(lane < 16 && cnode[lane & 15] == nl);
      while (cm_ != 0ull) {
        const int w = __builtin_ctzll(cm_);
        cm_ &= cm_ - 1ull;
        const float4 c = *reinterpret_cast<const float4*>(car + w * HH + lane * 4);
        v.x += c.x; v.y += c.y; v.z += c.z; v.w += c.w;
      }
      const float s0 = v.x * NEG_LN2, s1 = v.y * NEG_LN2, s2 = v.z * NEG_LN2, s3 = v.w * NEG_LN2;
      const unsigned short h0 = f2bf(s0), h1 = f2bf(s1), h2 = f2bf(s2), h3 = f2bf(s3);
      uint2 hv, lv;
      hv.x = (unsigned)h0 | ((unsigned)h1 << 16);
      hv.y = (unsigned)h2 | ((unsigned)h3 << 16);
      lv.x = pack2(s0 - bf2f(h0), s1 - bf2f(h1));
      lv.y = pack2(s2 - bf2f(h2), s3 - bf2f(h3));
      __builtin_amdgcn_wave_barrier();
      *reinterpret_cast<uint2*>(row + lane * 8) = hv;
      *reinterpret_cast<uint2*>(row + 512 + lane * 8) = lv;
    }
  }
  __syncthreads();

  for (int ntile = wave >> 3; ntile < (NPB + 15) / 16; ntile += 2) {
    f32x4 acc = {0.f, 0.f, 0.f, 0.f};
    const int srow = ntile * 16 + (lane & 15);
#pragma unroll
    for (int ks = 0; ks < 8; ++ks) {
      const bf16x8 bh = *reinterpret_cast<const bf16x8*>(QS + srow * QS_STRIDE + ks * 64 + (lane >> 4) * 16);
      const bf16x8 bl = *reinterpret_cast<const bf16x8*>(QS + srow * QS_STRIDE + 512 + ks * 64 + (lane >> 4) * 16);
      acc = __builtin_amdgcn_mfma_f32_16x16x32_bf16(wf[ks], bh, acc, 0, 0, 0);
      acc = __builtin_amdgcn_mfma_f32_16x16x32_bf16(wf[ks], bl, acc, 0, 0, 0);
    }
    if (srow < NPB) {
      const int oc = oct * 16 + (lane >> 4) * 4;
      const float dg = (float)degs[srow];
      uint2 o;
      o.x = pack2(acc[0] + dg * bf2f(b2[oc + 0]), acc[1] + dg * bf2f(b2[oc + 1]));
      o.y = pack2(acc[2] + dg * bf2f(b2[oc + 2]), acc[3] + dg * bf2f(b2[oc + 3]));
      *reinterpret_cast<uint2*>(out + (size_t)(n0 + srow) * DD + oc) = o;
    }
  }
}

}

extern "C" void kernel_launch(void* const* d_in, const int* in_sizes, int n_in,
                              void* d_out, int out_size, void* d_ws, size_t ws_size,
                              hipStream_t stream) {
  const unsigned short* x  = (const unsigned short*)d_in[0];
  const unsigned short* ea = (const unsigned short*)d_in[1];
  const int* src = (const int*)d_in[2];
  const int* dst = (const int*)d_in[3];
  const unsigned short* w1 = (const unsigned short*)d_in[4];
  const unsigned short* b1 = (const unsigned short*)d_in[5];
  const unsigned short* w2 = (const unsigned short*)d_in[6];
  const unsigned short* b2 = (const unsigned short*)d_in[7];
  unsigned char* ws = (unsigned char*)d_ws;
  unsigned short* Pp = (unsigned short*)(ws + WS_P);
  unsigned short* Qp = (unsigned short*)(ws + WS_Q);
  uint2* bk = (uint2*)(ws + WS_BK);
  unsigned* cntg = (unsigned*)(ws + WS_CNT);
  unsigned short* w2T = (unsigned short*)(ws + WS_W2T);

  k1_prep<<<K1_BLOCKS, 1024, 0, stream>>>(x, ea, src, dst, w1, b1, w2, Pp, Qp, bk, cntg, w2T);
  k2_agg<<<NB, 1024, 0, stream>>>(Pp, Qp, bk, cntg, w2T, w1, b2, ea, src, dst, (unsigned short*)d_out);
}
```

```cpp
#include <hip/hip_runtime.h>
#include <hip/hip_bf16.h>
#include <stdint.h>

using bf16x8 = __attribute__((ext_vector_type(8))) short;
using s16x4  = __attribute__((ext_vector_type(4))) short;
using f32x4  = __attribute__((ext_vector_type(4))) float;
typedef __attribute__((address_space(3))) s16x4* lds_s16x4_ptr;

namespace {

constexpr int NN  = 10000;
constexpr int EE  = 320000;
constexpr int DD  = 128;
constexpr int HH  = 256;
constexpr int G   = 64;
constexpr int CH  = EE / G;
constexpr int NPB = 20;
constexpr int NB  = NN / NPB;
constexpr int CAP = 32;
constexpr int SPT = CAP / 16;
constexpr int NPW = (NPB + 15) / 16;
constexpr int GEMM_BLOCKS = 160;
constexpr int K1_BLOCKS = G + GEMM_BLOCKS;
constexpr int W_STRIDE = 288;
constexpr int O_STRIDE = 272;

constexpr size_t WS_P    = 0;
constexpr size_t WS_Q    = WS_P + (size_t)(NN + 1) * HH * 2;
constexpr size_t WS_BK   = WS_Q + (size_t)NN * HH * 2;
constexpr size_t WS_CNT  = WS_BK + (size_t)NB * G * CAP * 8;
constexpr size_t WS_W2T  = WS_CNT + (size_t)NB * G * 4;
constexpr size_t WS_END  = WS_W2T + (size_t)DD * HH * 2;

constexpr float NEG_LOG2E = -1.4426950408889634f;
constexpr float NEG_LN2   = -0.6931471805599453f;
constexpr unsigned NULL_REC = (unsigned)NN;
constexpr unsigned short NULL_VAL = 0x7149;

__device__ __forceinline__ float bf2f(unsigned short u) { return __uint_as_float(((unsigned)u) << 16); }
__device__ __forceinline__ unsigned short f2bf(float f) {
  __hip_bfloat16 h = __float2bfloat16(f);
  return *reinterpret_cast<unsigned short*>(&h);
}
__device__ __forceinline__ unsigned pack2(float a, float b) {
  return (unsigned)f2bf(a) | ((unsigned)f2bf(b) << 16);
}

__device__ __forceinline__ void store_wt(__amdgpu_buffer_rsrc_t rsrc, int byte_off, uint4 v) {
  typedef unsigned u32x4v __attribute__((ext_vector_type(4)));
  const u32x4v vv = {v.x, v.y, v.z, v.w};
  __builtin_amdgcn_raw_buffer_store_b128(vv, rsrc, byte_off, 0, 16);
}

constexpr int K1_W = 0;
constexpr int K1_O = DD * W_STRIDE;
constexpr int K1_LDS = K1_O + 8 * 16 * O_STRIDE;

__global__ __launch_bounds__(1024) void k1_prep(
    const unsigned short* __restrict__ x, const unsigned short* __restrict__ eattr,
    const int* __restrict__ src, const int* __restrict__ dst,
    const unsigned short* __restrict__ w1, const unsigned short* __restrict__ b1,
    const unsigned short* __restrict__ w2,
    unsigned short* __restrict__ Pp, unsigned short* __restrict__ Qp,
    uint2* __restrict__ bk, unsigned* __restrict__ cntg,
    unsigned short* __restrict__ w2T) {
  __shared__ __attribute__((aligned(16))) unsigned char smem[K1_LDS];
  const int tid = threadIdx.x;
  const int lane = tid & 63;
  const int wave = tid >> 6;
  const int bid = blockIdx.x;

  if (bid < G) {
    unsigned* hist = reinterpret_cast<unsigned*>(smem);
    const int ebase = bid * CH;
    int d[5];
    unsigned rec[5];
#pragma unroll
    for (int i = 0; i < 5; ++i) {
      const int idx = i * 1024 + tid;
      d[i] = 0; rec[i] = 0;
      if (idx < CH) {
        d[i] = dst[ebase + idx];
        rec[i] = ((unsigned)src[ebase + idx] & 0xffffu) | ((unsigned)eattr[ebase + idx] << 16);
      }
    }
    if (tid < 512) hist[tid] = 0u;
    __syncthreads();
#pragma unroll
    for (int i = 0; i < 5; ++i) {
      const int idx = i * 1024 + tid;
      if (idx < CH) {
        const int bin = d[i] / NPB;
        const int nib = d[i] - bin * NPB;
        const unsigned slot = atomicAdd(&hist[bin], 1u);
        if (slot < CAP) bk[((size_t)bin * G + bid) * CAP + slot] = make_uint2(((unsigned)nib << 16) | (unsigned)idx, rec[i]);
      }
    }
    __syncthreads();
    if (tid < NB) cntg[tid * G + bid] = hist[tid];
  } else {
    const int gb = bid - G;
    const int half = gb / 80;
    const int hh = (gb / 40) & 1;
    const int tg = gb % 40;
#pragma unroll
    for (int i = 0; i < 2; ++i) {
      const int k = (tid >> 4) + 64 * i;
      const int c8 = (tid & 15) * 8;
      const uint4 v = *reinterpret_cast<const uint4*>(w1 + (size_t)(half * DD + k) * HH + hh * 128 + c8);
      *reinterpret_cast<uint4*>(smem + K1_W + k * W_STRIDE + c8 * 2) = v;
    }
    if (gb < 64 && tid >= 512) {
      const int i = gb * 512 + tid - 512;
      const int k = i >> 7, oc = i & 127;
      w2T[oc * HH + k] = w2[i];
    }
    if (gb == 64 && tid >= 512 && tid < 512 + HH) Pp[(size_t)NN * HH + tid - 512] = NULL_VAL;
    const int nt0 = tg * 16 + wave * 2;
    const bool active = wave < 8 && nt0 < NN / 16;
    const int ntc0 = nt0 < NN / 16 ? nt0 : NN / 16 - 1;
    const int ntc1 = nt0 + 1 < NN / 16 ? nt0 + 1 : NN / 16 - 1;
    bf16x8 xb0[4], xb1[4];
    uint2 bv[8];
    if (active) {
#pragma unroll
      for (int ks = 0; ks < 4; ++ks) {
        xb0[ks] = *reinterpret_cast<const bf16x8*>(x + (size_t)(ntc0 * 16 + (lane & 15)) * DD + ks * 32 + (lane >> 4) * 8);
        xb1[ks] = *reinterpret_cast<const bf16x8*>(x + (size_t)(ntc1 * 16 + (lane & 15)) * DD + ks * 32 + (lane >> 4) * 8);
      }
#pragma unroll
      for (int ct = 0; ct < 8; ++ct) {
        bv[ct] = *reinterpret_cast<const uint2*>(b1 + hh * 128 + ct * 16 + (lane >> 4) * 4);
        if (!half) bv[ct] = make_uint2(0u, 0u);
      }
    }
    __syncthreads();
    if (active) {
      unsigned char* ob = smem + K1_O + wave * (16 * O_STRIDE);
      const unsigned lbase = (unsigned)(size_t)smem + K1_W + ((lane >> 4) * 8 + ((lane & 15) >> 2)) * W_STRIDE + (lane & 3) * 8;
      uint2 o1[8];
#pragma unroll
      for (int ct = 0; ct < 8; ++ct) {
        f32x4 acc0 = {__uint_as_float(bv[ct].x << 16), __uint_as_float(bv[ct].x & 0xffff0000u),
                      __uint_as_float(bv[ct].y << 16), __uint_as_float(bv[ct].y & 0xffff0000u)};
        f32x4 acc1 = acc0;
#pragma unroll
        for (int ks = 0; ks < 4; ++ks) {
          const unsigned a0 = lbase + (ks * 32) * W_STRIDE + ct * 32;
          const s16x4 lo = __builtin_amdgcn_ds_read_tr16_b64_v4i16((lds_s16x4_ptr)(size_t)a0);
          const s16x4 hi = __builtin_amdgcn_ds_read_tr16_b64_v4i16((lds_s16x4_ptr)(size_t)(a0 + 4 * W_STRIDE));
          const bf16x8 a = {lo[0], lo[1], lo[2], lo[3], hi[0], hi[1], hi[2], hi[3]};
          acc0 = __builtin_amdgcn_mfma_f32_16x16x32_bf16(a, xb0[ks], acc0, 0, 0, 0);
          acc1 = __builtin_amdgcn_mfma_f32_16x16x32_bf16(a, xb1[ks], acc1, 0, 0, 0);
        }
        uint2 o;
        o.x = pack2(acc0[0] * NEG_LOG2E, acc0[1] * NEG_LOG2E);
        o.y = pack2(acc0[2] * NEG_LOG2E, acc0[3] * NEG_LOG2E);
        *reinterpret_cast<uint2*>(ob + (lane & 15) * O_STRIDE + ct * 32 + (lane >> 4) * 8) = o;
        o1[ct].x = pack2(acc1[0] * NEG_LOG2E, acc1[1] * NEG_LOG2E);
        o1[ct].y = pack2(acc1[2] * NEG_LOG2E, acc1[3] * NEG_LOG2E);
      }
      const __amdgpu_buffer_rsrc_t wrs = __builtin_amdgcn_make_buffer_rsrc(half ? Qp : Pp, (short)0, (int)(NN * HH * 2), 0x00020000);
#pragma unroll
      for (int r = 0; r < 4; ++r) {
        const int row = r * 4 + (lane >> 4);
        const uint4 v = *reinterpret_cast<const uint4*>(ob + row * O_STRIDE + (lane & 15) * 16);
        store_wt(wrs, ((nt0 * 16 + row) * HH + hh * 128 + (lane & 15) * 8) * 2, v);
      }
      if (nt0 + 1 < NN / 16) {
        __builtin_amdgcn_wave_barrier();
#pragma unroll
        for (int ct = 0; ct < 8; ++ct)
          *reinterpret_cast<uint2*>(ob + (lane & 15) * O_STRIDE + ct * 32 + (lane >> 4) * 8) = o1[ct];
#pragma unroll
        for (int r = 0; r < 4; ++r) {
          const int row = r * 4 + (lane >> 4);
          const uint4 v = *reinterpret_cast<const uint4*>(ob + row * O_STRIDE + (lane & 15) * 16);
          store_wt(wrs, ((nt0 * 16 + 16 + row) * HH + hh * 128 + (lane & 15) * 8) * 2, v);
        }
      }
    }
  }
}

constexpr int QS_STRIDE = 1040;
constexpr int LCAP = 2048;
constexpr int K2_QS   = 0;
constexpr int K2_Q    = K2_QS + NPB * QS_STRIDE;
constexpr int K2_CAR  = K2_Q + NPB * HH * 2;
constexpr int K2_KEY  = K2_CAR + 16 * HH * 4;
constexpr int K2_CM   = K2_KEY + G * CAP * 4;
constexpr int K2_L    = K2_CM + NPB * G * 4;
constexpr int K2_DEG  = K2_L + (LCAP + 32) * 4;
constexpr int K2_RP   = K2_DEG + 64 * 4;
constexpr int K2_CN   = K2_RP + 64 * 4;
constexpr int K2_FLAG = K2_CN + 16 * 4 + 64 * 4;
constexpr int K2_LDS  = K2_FLAG + 16;

__device__ __forceinline__ float sig_t(float t) {
  return __builtin_amdgcn_rcpf(1.0f + __builtin_amdgcn_exp2f(t));
}

struct Acc { float a0, a1, a2, a3; };

__device__ __forceinline__ void edge_acc(Acc& a, unsigned rec, uint2 v, const float4& q,
                                         float wq0, float wq1, float wq2, float wq3) {
  const float e = __uint_as_float(rec & 0xffff0000u);
  const float t0 = fmaf(e, wq0, q.x) + __uint_as_float(v.x << 16);
  const float t1 = fmaf(e, wq1, q.y) + __uint_as_float(v.x & 0xffff0000u);
  const float t2 = fmaf(e, wq2, q.z) + __uint_as_float(v.y << 16);
  const float t3 = fmaf(e, wq3, q.w) + __uint_as_float(v.y & 0xffff0000u);
  a.a0 = fmaf(t0, sig_t(t0), a.a0);
  a.a1 = fmaf(t1, sig_t(t1), a.a1);
  a.a2 = fmaf(t2, sig_t(t2), a.a2);
  a.a3 = fmaf(t3, sig_t(t3), a.a3);
}

__device__ __forceinline__ float4 load_q(const unsigned char* qrow, int lane) {
  const uint2 v = *reinterpret_cast<const uint2*>(qrow + lane * 8);
  return make_float4(__uint_as_float(v.x << 16), __uint_as_float(v.x & 0xffff0000u),
                     __uint_as_float(v.y << 16), __uint_as_float(v.y & 0xffff0000u));
}

__device__ __forceinline__ int wave_incl_scan(int v, int lane) {
#pragma unroll
  for (int o = 1; o < 64; o <<= 1) {
    const int t = __shfl_up(v, o, 64);
    if (lane >= o) v += t;
  }
  return v;
}

struct Grp { unsigned r0, r1, r2, r3; uint2 v0, v1, v2, v3; };

__device__ __forceinline__ uint2 gather_row(__amdgpu_buffer_rsrc_t rsrc, unsigned loff, unsigned rec) {
  typedef unsigned u32x2v __attribute__((ext_vector_type(2)));
  const u32x2v v = __builtin_amdgcn_raw_buffer_load_b64(rsrc, (int)loff, (int)((rec & 0xffffu) * (HH * 2)), 0);
  return make_uint2(v[0], v[1]);
}

__global__ __launch_bounds__(1024, 8) void k2_agg(
    const unsigned short* __restrict__ Pp, const unsigned short* __restrict__ Qp,
    const uint2* __restrict__ bk, const unsigned* __restrict__ cntg,
    const unsigned short* __restrict__ w2T, const unsigned short* __restrict__ w1,
    const unsigned short* __restrict__ b2,
    const unsigned short* __restrict__ eattr, const int* __restrict__ src, const int* __restrict__ dst,
    unsigned short* __restrict__ out) {
  __shared__ __attribute__((aligned(16))) unsigned char smem[K2_LDS];
  unsigned char* QS = smem + K2_QS;
  unsigned char* Qs = smem + K2_Q;
  float* car = reinterpret_cast<float*>(smem + K2_CAR);
  unsigned* keys = reinterpret_cast<unsigned*>(smem + K2_KEY);
  unsigned* cm = reinterpret_cast<unsigned*>(smem + K2_CM);
  unsigned* L = reinterpret_cast<unsigned*>(smem + K2_L);
  int* degs = reinterpret_cast<int*>(smem + K2_DEG);
  int* rps = reinterpret_cast<int*>(smem + K2_RP);
  int* cnode = reinterpret_cast<int*>(smem + K2_CN);
  int* flag = reinterpret_cast<int*>(smem + K2_FLAG);

  const int tid = threadIdx.x;
  const int lane = tid & 63;
  const int wave = __builtin_amdgcn_readfirstlane(tid >> 6);
  const int n0 = blockIdx.x * NPB;

  const int mych = tid >> 4;
  const int mysl = tid & 15;
  const unsigned mycnt = cntg[blockIdx.x * G + mych];
  uint2 rr4[SPT];
  {
    const uint2* bp = bk + ((size_t)blockIdx.x * G + mych) * CAP + mysl;
#pragma unroll
    for (int j = 0; j < SPT; ++j) rr4[j] = bp[16 * j];
  }
  {
    const uint4* qg = reinterpret_cast<const uint4*>(Qp + (size_t)n0 * HH);
    for (int i = tid; i < NPB * HH / 8; i += 1024) reinterpret_cast<uint4*>(Qs)[i] = qg[i];
  }
  float wq0, wq1, wq2, wq3;
  {
    const uint2 wv = *reinterpret_cast<const uint2*>(w1 + (size_t)(2 * DD) * HH + lane * 4);
    wq0 = __uint_as_float(wv.x << 16) * NEG_LOG2E;
    wq1 = __uint_as_float(wv.x & 0xffff0000u) * NEG_LOG2E;
    wq2 = __uint_as_float(wv.y << 16) * NEG_LOG2E;
    wq3 = __uint_as_float(wv.y & 0xffff0000u) * NEG_LOG2E;
  }
  for (int i = tid; i < NPB * G; i += 1024) cm[i] = 0u;
  if (tid == 0) *flag = 0;
  if (tid < 16) cnode[tid] = -1;
  __syncthreads();

  unsigned rk[SPT], rv[SPT];
#pragma unroll
  for (int j = 0; j < SPT; ++j) {
    const bool ok = (unsigned)(mysl + 16 * j) < mycnt;
    rk[j] = ok ? rr4[j].x : 0xffffffffu;
    rv[j] = rr4[j].y;
  }
  if (mycnt > (unsigned)CAP) *flag = 1;
#pragma unroll
  for (int j = 0; j < SPT; ++j) {
    keys[mych * CAP + mysl + 16 * j] = rk[j];
    if (rk[j] != 0xffffffffu && mycnt <= (unsigned)CAP) atomicAdd(&cm[(rk[j] >> 16) * G + mych], 1u);
  }
  __syncthreads();

#pragma unroll
  for (int s = 0; s < NPW; ++s) {
    const int nl = wave + 16 * s;
    if (nl < NPB) {
      const int c = (int)cm[nl * G + lane];
      const int incl = wave_incl_scan(c, lane);
      cm[nl * G + lane] = (unsigned)(incl - c);
      degs[nl] = __builtin_amdgcn_readlane(incl, 63);
    }
  }
  __syncthreads();

  const int mydeg = lane < NPB ? degs[lane] : 0;
  const int pincl = wave_incl_scan(mydeg, lane);
  const int rp = pincl - mydeg;
  const int T = __builtin_amdgcn_readlane(pincl, 63);
  const bool fast = (T <= LCAP) && (__builtin_amdgcn_readfirstlane(*flag) == 0);

  if (fast) {
    if (wave == 1) rps[lane] = rp;
    {
      unsigned mx = mycnt;
#pragma unroll
      for (int o = 16; o < 64; o <<= 1) mx = max(mx, (unsigned)__shfl_xor((int)mx, o, 64));
      const int nrow = __builtin_amdgcn_readfirstlane((int)((mx + 15u) >> 4));
      unsigned rank[SPT];
#pragma unroll
      for (int j = 0; j < SPT; ++j) rank[j] = 0u;
      const int nq = __builtin_amdgcn_readfirstlane((int)((mx + 3u) >> 2));
      for (int s4 = 0; s4 < nq; ++s4) {
        const uint4 kq = *reinterpret_cast<const uint4*>(keys + mych * CAP + s4 * 4);
#pragma unroll
        for (int j = 0; j < SPT; ++j) {
          if (j < nrow) {
            const unsigned kb = rk[j] & 0xffff0000u, kl = rk[j] & 0xffffu;
            rank[j] += (unsigned)((kq.x - kb) < kl) + (unsigned)((kq.y - kb) < kl) +
                       (unsigned)((kq.z - kb) < kl) + (unsigned)((kq.w - kb) < kl);
          }
        }
      }
#pragma unroll
      for (int j = 0; j < SPT; ++j) {
        if (j < nrow) {
          const unsigned nib = min(rk[j] >> 16, (unsigned)(NPB - 1));
          const int base = __shfl(rp, (int)nib, 64);
          if (rk[j] != 0xffffffffu) L[base + cm[nib * G + mych] + rank[j]] = rv[j];
        }
      }
    }
#pragma unroll
    for (int s = 0; s < NPW; ++s) {
      const int nl = wave + 16 * s;
      if (nl < NPB) {
        const int dg = __builtin_amdgcn_readlane(mydeg, nl);
        if (dg == 0) *reinterpret_cast<float4*>(QS + nl * QS_STRIDE + lane * 16) = make_float4(0.f, 0.f, 0.f, 0.f);
      }
    }
    if (wave == 0 && lane < 20) L[T + lane] = NULL_REC;
    __syncthreads();

    const int TG = (T + 3) >> 2;
    const int G0 = (wave * TG) >> 4;
    const int G1 = ((wave + 1) * TG) >> 4;
    if (G0 < G1) {
      int ni = __builtin_popcountll(__ballot(lane < NPB && rp <= G0 * 4)) - 1;
      bool mid = __builtin_amdgcn_readlane(rp, ni) < G0 * 4;
      int ende = ni + 1 < NPB ? __builtin_amdgcn_readlane(rp, ni + 1) : 0x7fffffff;
      if (ende >= T) ende = 0x7fffffff;
      float4 q = load_q(Qs + ni * (HH * 2), lane);
      Acc acc = {0.f, 0.f, 0.f, 0.f};
      const unsigned loff = (unsigned)lane * 8u;
      const __amdgpu_buffer_rsrc_t prsrc = __builtin_amdgcn_make_buffer_rsrc(
          const_cast<unsigned short*>(Pp), (short)0, (int)((NN + 1) * HH * 2), 0x00020000);
      unsigned rcur = L[G0 * 4 + lane];
      unsigned rnxt = L[min(G0 * 4 + 64 + lane, LCAP + 31)];

      auto flush = [&]() {
        float* dstrow = mid ? (car + wave * HH) : reinterpret_cast<float*>(QS + ni * QS_STRIDE);
        *reinterpret_cast<float4*>(dstrow + lane * 4) = make_float4(acc.a0, acc.a1, acc.a2, acc.a3);
        if (mid) cnode[wave] = ni;
        mid = false;
        acc.a0 = 0.f; acc.a1 = 0.f; acc.a2 = 0.f; acc.a3 = 0.f;
      };
      auto issue = [&](Grp& b, int g) {
        const int rel = (g - G0) * 4;
        if ((rel & 63) == 0 && rel > 0) {
          rcur = rnxt;
          rnxt = L[min(G0 * 4 + rel + 64 + lane, LCAP + 31)];
        }
        b.r0 = __builtin_amdgcn_readlane(rcur, (rel & 63) + 0);
        b.r1 = __builtin_amdgcn_readlane(rcur, (rel & 63) + 1);
        b.r2 = __builtin_amdgcn_readlane(rcur, (rel & 63) + 2);
        b.r3 = __builtin_amdgcn_readlane(rcur, (rel & 63) + 3);
        b.v0 = gather_row(prsrc, loff, b.r0);
        b.v1 = gather_row(prsrc, loff, b.r1);
        b.v2 = gather_row(prsrc, loff, b.r2);
        b.v3 = gather_row(prsrc, loff, b.r3);
      };
      auto boundary = [&](int e) {
        if (e == ende) {
          flush();
          do {
            ++ni;
            ende = __builtin_amdgcn_readfirstlane(ni + 1 < NPB ? rps[ni + 1] : T);
          } while (e == ende);
          if (ende >= T) ende = 0x7fffffff;
          q = load_q(Qs + ni * (HH * 2), lane);
        }
      };
      auto compute = [&](const Grp& b, int g) {
        boundary(g * 4 + 0); edge_acc(acc, b.r0, b.v0, q, wq0, wq1, wq2, wq3);
        boundary(g * 4 + 1); edge_acc(acc, b.r1, b.v1, q, wq0, wq1, wq2, wq3);
        boundary(g * 4 + 2); edge_acc(acc, b.r2, b.v2, q, wq0, wq1, wq2, wq3);
        boundary(g * 4 + 3); edge_acc(acc, b.r3, b.v3, q, wq0, wq1, wq2, wq3);
      };

      Grp A, B, C;
      issue(A, G0);
      __builtin_amdgcn_sched_barrier(0);
      issue(B, G0 + 1);
      __builtin_amdgcn_sched_barrier(0);
      for (int g = G0; g < G1; g += 3) {
        issue(C, g + 2); compute(A, g);
        issue(A, g + 3); if (g + 1 < G1) compute(B, g + 1);
        issue(B, g + 4); if (g + 2 < G1) compute(C, g + 2);
      }
      flush();
    }
  } else {
#pragma unroll 1
    for (int s = 0; s < NPW; ++s) {
      const int nl = wave + 16 * s;
      if (nl < NPB) {
        const float4 q = load_q(Qs + nl * (HH * 2), lane);
        Acc acc = {0.f, 0.f, 0.f, 0.f};
        int total = 0;
        for (int e0 = 0; e0 < EE; e0 += 64) {
          const bool hit = dst[e0 + lane] == n0 + nl;
          unsigned long long m = __ballot(hit);
          if (m != 0ull) {
            const unsigned rec = hit ? (((unsigned)src[e0 + lane] & 0xffffu) | ((unsigned)eattr[e0 + lane] << 16)) : NULL_REC;
            total += __builtin_popcountll(m);
            while (m != 0ull) {
              const int i = __builtin_ctzll(m);
              m &= m - 1ull;
              const unsigned rr = __builtin_amdgcn_readlane(rec, i);
              const uint2 v = *reinterpret_cast<const uint2*>(Pp + (size_t)(rr & 0xffffu) * HH + lane * 4);
              edge_acc(acc, rr, v, q, wq0, wq1, wq2, wq3);
            }
          }
        }
        *reinterpret_cast<float4*>(QS + nl * QS_STRIDE + lane * 16) = make_float4(acc.a0, acc.a1, acc.a2, acc.a3);
        degs[nl] = total;
      }
    }
  }

  const int oct = wave & 7;
  bf16x8 wf[8];
#pragma unroll
  for (int ks = 0; ks < 8; ++ks)
    wf[ks] = *reinterpret_cast<const bf16x8*>(w2T + (oct * 16 + (lane & 15)) * HH + ks * 32 + (lane >> 4) * 8);
  __syncthreads();

#pragma unroll
  for (int s = 0; s < NPW; ++s) {
    const int nl = wave + 16 * s;
    if (nl < NPB) {
      unsigned char* row = QS + nl * QS_STRIDE;
      float4 v = *reinterpret_cast<const float4*>(row + lane * 16);
      unsigned long long cm_ = __ballot(lane < 16 && cnode[lane & 15] == nl);
      while (cm_ != 0ull) {
        const int w = __builtin_ctzll(cm_);
        cm_ &= cm_ - 1ull;
        const float4 c = *reinterpret_cast<const float4*>(car + w * HH + lane * 4);
        v.x += c.x; v.y += c.y; v.z += c.z; v.w += c.w;
      }
      const float s0 = v.x * NEG_LN2, s1 = v.y * NEG_LN2, s2 = v.z * NEG_LN2, s3 = v.w * NEG_LN2;
      const unsigned short h0 = f2bf(s0), h1 = f2bf(s1), h2 = f2bf(s2), h3 = f2bf(s3);
      uint2 hv, lv;
      hv.x = (unsigned)h0 | ((unsigned)h1 << 16);
      hv.y = (unsigned)h2 | ((unsigned)h3 << 16);
      lv.x = pack2(s0 - bf2f(h0), s1 - bf2f(h1));
      lv.y = pack2(s2 - bf2f(h2), s3 - bf2f(h3));
      __builtin_amdgcn_wave_barrier();
      *reinterpret_cast<uint2*>(row + lane * 8) = hv;
      *reinterpret_cast<uint2*>(row + 512 + lane * 8) = lv;
    }
  }
  __syncthreads();

  for (int ntile = wave >> 3; ntile < (NPB + 15) / 16; ntile += 2) {
    f32x4 acc = {0.f, 0.f, 0.f, 0.f};
    const int srow = ntile * 16 + (lane & 15);
#pragma unroll
    for (int ks = 0; ks < 8; ++ks) {
      const bf16x8 bh = *reinterpret_cast<const bf16x8*>(QS + srow * QS_STRIDE + ks * 64 + (lane >> 4) * 16);
      const bf16x8 bl = *reinterpret_cast<const bf16x8*>(QS + srow * QS_STRIDE + 512 + ks * 64 + (lane >> 4) * 16);
      acc = __builtin_amdgcn_mfma_f32_16x16x32_bf16(wf[ks], bh, acc, 0, 0, 0);
      acc = __builtin_amdgcn_mfma_f32_16x16x32_bf16(wf[ks], bl, acc, 0, 0, 0);
    }
    if (srow < NPB) {
      const int oc = oct * 16 + (lane >> 4) * 4;
      const float dg = (float)degs[srow];
      uint2 o;
      o.x = pack2(acc[0] + dg * bf2f(b2[oc + 0]), acc[1] + dg * bf2f(b2[oc + 1]));
      o.y = pack2(acc[2] + dg * bf2f(b2[oc + 2]), acc[3] + dg * bf2f(b2[oc + 3]));
      *reinterpret_cast<uint2*>(out + (size_t)(n0 + srow) * DD + oc) = o;
    }
  }
}

}

extern "C" void kernel_launch(void* const* d_in, const int* in_sizes, int n_in,
                              void* d_out, int out_size, void* d_ws, size_t ws_size,
                              hipStream_t stream) {
  const unsigned short* x  = (const unsigned short*)d_in[0];
  const unsigned short* ea = (const unsigned short*)d_in[1];
  const int* src = (const int*)d_in[2];
  const int* dst = (const int*)d_in[3];
  const unsigned short* w1 = (const unsigned short*)d_in[4];
  const unsigned short* b1 = (const unsigned short*)d_in[5];
  const unsigned short* w2 = (const unsigned short*)d_in[6];
  const unsigned short* b2 = (const unsigned short*)d_in[7];
  unsigned char* ws = (unsigned char*)d_ws;
  unsigned short* Pp = (unsigned short*)(ws + WS_P);
  unsigned short* Qp = (unsigned short*)(ws + WS_Q);
  uint2* bk = (uint2*)(ws + WS_BK);
  unsigned* cntg = (unsigned*)(ws + WS_CNT);
  unsigned short* w2T = (unsigned short*)(ws + WS_W2T);

  k1_prep<<<K1_BLOCKS, 1024, 0, stream>>>(x, ea, src, dst, w1, b1, w2, Pp, Qp, bk, cntg, w2T);
  k2_agg<<<NB, 1024, 0, stream>>>(Pp, Qp, bk, cntg, w2T, w1, b2, ea, src, dst, (unsigned short*)d_out);
}
```
